```python
import torch

def _softmax(x, axis=-1):
    return torch.softmax(x, dim=axis)
def _gelu(x):
    return torch.nn.functional.gelu(x, approximate="tanh")
def _silu(x):
    return torch.nn.functional.silu(x)
def _sigmoid(x):
    return torch.sigmoid(x)
def _layernorm(x, axis=-1, eps=1e-5):
    m = x.mean(dim=axis, keepdim=True)
    v = x.var(dim=axis, keepdim=True, unbiased=False)
    return (x - m) / torch.sqrt(v + eps)
def _scatter_add(tgt, idx, src, axis=0):
    return tgt.clone().index_add_(axis, idx, src)
def _segment_sum(src, seg, n):
    out = torch.zeros((n,) + src.shape[1:], dtype=src.dtype)
    return out.index_add_(0, seg, src)
def _pad(x, pw):
    flat = tuple(v for pair in reversed(pw) for v in pair)
    return torch.nn.functional.pad(x, flat)

kernel_name = 'sequential_preLN__sliding_local_dilated__glu_gated_geglu__prompt'

def setup_inputs():

    _w = (lambda a: (lambda t: t.to(torch.bfloat16) if t.is_floating_point() else t)(torch.as_tensor(a)))
    def _A_setup():
        B, S, D = 1, 2048, 512
        return {
            "x": _w((lambda *s: torch.randn(*s, dtype=torch.bfloat16) * 0.05)(B, S, D)),
            "wqkv": _w((lambda *s: torch.randn(*s, dtype=torch.bfloat16) * 0.05)(D, 3 * D)),
            "wo": _w((lambda *s: torch.randn(*s, dtype=torch.bfloat16) * 0.05)(D, D)),
        }
    _A_in = _A_setup()

    def _B_setup():
        B, S, Dm, Df = 1, 2048, 512, 1792
        return {
            "x": _w((lambda *s: torch.randn(*s, dtype=torch.bfloat16) * 0.05)(B, S, Dm)),
            "w_gate": _w((lambda *s: torch.randn(*s, dtype=torch.bfloat16) * 0.8)(Dm, Df) * (Dm ** -0.5)),
            "w_up": _w((lambda *s: torch.randn(*s, dtype=torch.bfloat16) * 0.8)(Dm, Df) * (Dm ** -0.5)),
            "w_down": _w((lambda *s: torch.randn(*s, dtype=torch.bfloat16) * 0.8)(Df, Dm) * (Df ** -0.5)),
        }
    _B_in = _B_setup()

    return {
        "A_x": _A_in["x"],
        "A_wqkv": _A_in["wqkv"],
        "A_wo": _A_in["wo"],
        "B_x": _B_in["x"],
        "B_w_gate": _B_in["w_gate"],
        "B_w_up": _B_in["w_up"],
        "B_w_down": _B_in["w_down"],
    }

def reference(A_x, A_wqkv, A_wo, B_x, B_w_gate, B_w_up, B_w_down):
    def _A_ref(x, wqkv, wo):
        B, S, D = x.shape
        H = 32; Dh = D // H
        W, dil = 8, 2
        qkv = (lambda x, *d: x.permute(*d))((x @ wqkv).reshape(B, S, 3, H, Dh), 2, 0, 3, 1, 4)
        q, k, v = qkv[0], qkv[1], qkv[2]
        scale = Dh ** -0.5
        pos = torch.arange(S)
        off = torch.arange(W) * dil
        idx = pos[:, None] - off[None, :]
        mask = idx >= 0
        idx_c = torch.where(mask, idx, 0).reshape(-1)
        kg = k[:, :, idx_c, :].reshape(B, H, S, W, Dh)
        vg = v[:, :, idx_c, :].reshape(B, H, S, W, Dh)
        logits = torch.einsum("bhsd,bhswd->bhsw", q, kg) * scale
        logits = torch.where(mask[None, None, :, :], logits, -1e9)
        attn = _softmax(logits, axis=-1)
        o = torch.einsum("bhsw,bhswd->bhsd", attn, vg)
        return (lambda x, *d: x.permute(*d))(o, 0, 2, 1, 3).reshape(B, S, D) @ wo

    def _B_ref(x, w_gate, w_up, w_down):
        g = torch.einsum("bsd,df->bsf", x, w_gate)
        u = torch.einsum("bsd,df->bsf", x, w_up)
        h = _gelu(g) * u
        return torch.einsum("bsf,fd->bsd", h, w_down)

    x = A_x
    h = x + _A_ref(_layernorm(x, axis=-1), A_wqkv, A_wo)
    h2 = h + _B_ref(_layernorm(h, axis=-1), B_w_gate, B_w_up, B_w_down)
    return h2
```

```cpp
#include <hip/hip_runtime.h>
#include <hip/hip_bf16.h>
typedef unsigned short u16;
__device__ __forceinline__ float bf2f(u16 b){ return __builtin_bit_cast(float, (unsigned)b<<16); }
__device__ __forceinline__ u16 f2bf(float f){ __hip_bfloat16 h = __float2bfloat16(f); return __builtin_bit_cast(u16,h); }
constexpr int S=2048, D=512, H=32, DH=16, W=8, DIL=2, DF=1792;

__global__ void k_ln(const u16* x, u16* y){
  int row = blockIdx.x; int l = threadIdx.x;
  float v[8]; float s=0;
  for(int j=0;j<8;j++){ v[j]=bf2f(x[row*D + l*8+j]); s+=v[j]; }
  for(int o=32;o;o>>=1) s+=__shfl_xor(s,o);
  float m=s/D; float q=0;
  for(int j=0;j<8;j++){ float d=v[j]-m; q+=d*d; }
  for(int o=32;o;o>>=1) q+=__shfl_xor(q,o);
  float r=rsqrtf(q/D+1e-5f);
  for(int j=0;j<8;j++) y[row*D+l*8+j]=f2bf((v[j]-m)*r);
}
__global__ void k_gemm(const u16* A, const u16* Wt, u16* C, const u16* res, int M,int N,int K){
  int n = blockIdx.x*64+threadIdx.x; int m = blockIdx.y;
  float acc=0;
  for(int k=0;k<K;k++) acc += bf2f(A[m*K+k])*bf2f(Wt[(size_t)k*N+n]);
  if(res) acc = bf2f(f2bf(acc)) + bf2f(res[m*N+n]);
  C[(size_t)m*N+n]=f2bf(acc);
}
__global__ void k_attn(const u16* qkv, u16* o){
  int s = blockIdx.x; int h = threadIdx.x; if(h>=H) return;
  float q[DH]; for(int d=0;d<DH;d++) q[d]=bf2f(qkv[s*3*D + h*DH+d]);
  float lg[W]; float mx=-1e30f;
  for(int w=0;w<W;w++){ int p=s-w*DIL; float a=0; if(p>=0){ for(int d=0;d<DH;d++) a+=q[d]*bf2f(qkv[p*3*D + D + h*DH+d]); a*=0.25f; } else a=-1e9f; lg[w]=a; mx=fmaxf(mx,a);}
  float sum=0; for(int w=0;w<W;w++){ lg[w]=__expf(lg[w]-mx); sum+=lg[w]; }
  for(int d=0;d<DH;d++){ float a=0; for(int w=0;w<W;w++){ int p=s-w*DIL; if(p<0) p=0; a+=lg[w]*bf2f(qkv[p*3*D+2*D+h*DH+d]); } o[s*D+h*DH+d]=f2bf(a/sum); }
}
__global__ void k_geglu(const u16* g,const u16* u,u16* hh,int n){
  int i=blockIdx.x*256+threadIdx.x; if(i>=n) return;
  float x=bf2f(g[i]); float z=0.7978845608f*(x+0.044715f*x*x*x); float ge=0.5f*x*(1.f+tanhf(z));
  hh[i]=f2bf(bf2f(f2bf(ge))*bf2f(u[i]));
}
extern "C" void kernel_launch(void* const* d_in, const int* in_sizes, int n_in, void* d_out, int out_size, void* d_ws, size_t ws_size, hipStream_t stream){
  const u16* x=(const u16*)d_in[0]; const u16* wqkv=(const u16*)d_in[1]; const u16* wo=(const u16*)d_in[2];
  const u16* wg=(const u16*)d_in[4]; const u16* wu=(const u16*)d_in[5]; const u16* wd=(const u16*)d_in[6];
  u16* ws=(u16*)d_ws; u16* xn=ws; u16* qkv=xn+S*D; u16* o=qkv+S*3*D; u16* h=o+S*D; u16* hn=h+S*D; u16* g=hn+S*D; u16* u=g+S*DF; u16* hh=u+S*DF;
  k_ln<<<S,64,0,stream>>>(x,xn);
  k_gemm<<<dim3(3*D/64,S),64,0,stream>>>(xn,wqkv,qkv,nullptr,S,3*D,D);
  k_attn<<<S,64,0,stream>>>(qkv,o);
  k_gemm<<<dim3(D/64,S),64,0,stream>>>(o,wo,h,x,S,D,D);
  k_ln<<<S,64,0,stream>>>(h,hn);
  k_gemm<<<dim3(DF/64,S),64,0,stream>>>(hn,wg,g,nullptr,S,DF,D);
  k_gemm<<<dim3(DF/64,S),64,0,stream>>>(hn,wu,u,nullptr,S,DF,D);
  k_geglu<<<S*DF/256,256,0,stream>>>(g,u,hh,S*DF);
  k_gemm<<<dim3(D/64,S),64,0,stream>>>(hh,wd,(u16*)d_out,h,S,D,DF);
}
```

```cpp
#include <hip/hip_runtime.h>
#include <hip/hip_bf16.h>
#define LAS __attribute__((address_space(3)))
#define GAS __attribute__((address_space(1)))
typedef unsigned short u16; typedef unsigned u32;
typedef short bf16x8 __attribute__((ext_vector_type(8)));
typedef short bf16x4 __attribute__((ext_vector_type(4)));
typedef float f32x4 __attribute__((ext_vector_type(4)));
typedef float f32x2 __attribute__((ext_vector_type(2)));
typedef unsigned u32x4 __attribute__((ext_vector_type(4)));
typedef unsigned u32x2 __attribute__((ext_vector_type(2)));
typedef __bf16 bf2v __attribute__((ext_vector_type(2)));
typedef __amdgpu_buffer_rsrc_t rsrc_t;
#define DEV __device__ __forceinline__

constexpr int S=2048, D=512, DF=1792;
constexpr float EPS=1e-5f;
constexpr size_t WS_FLAGS=0;
constexpr size_t WS_O   = 65536;
constexpr size_t WS_H   = WS_O + (size_t)S*D*2;
constexpr size_t WS_ST  = WS_H + (size_t)S*D*2;
constexpr size_t WS_HH  = WS_ST + (size_t)S*16*8;
constexpr size_t WS_END = WS_HH + (size_t)S*DF*2;

DEV rsrc_t mkrsrc(const void* p, u32 bytes){ return __builtin_amdgcn_make_buffer_rsrc((void*)p, 0, bytes, 0x00020000); }
DEV void dma(rsrc_t rs, u32 voff, u32 soff, u32 lds){
  u32 keep;
  asm volatile("s_mov_b32 %0, m0\n\ts_mov_b32 m0, %4\n\ts_nop 0\n\tbuffer_load_dwordx4 %1, %2, %3 offen lds\n\ts_mov_b32 m0, %0"
    : "=&s"(keep) : "v"(voff), "s"(rs), "s"(soff), "s"(lds) : "memory");
}
#define WAITV(n) asm volatile("s_waitcnt vmcnt(" #n ")" ::: "memory")
#define WAITL0() asm volatile("s_waitcnt lgkmcnt(0)" ::: "memory")
#define BAR() do{ asm volatile("s_waitcnt lgkmcnt(0)" ::: "memory"); __builtin_amdgcn_s_barrier(); asm volatile("" ::: "memory"); }while(0)
template<int N> DEV void waitv(){ asm volatile("s_waitcnt vmcnt(%0)" :: "n"(N) : "memory"); }
DEV bf16x8 trfrag(u32 a0, u32 a1){
  bf16x4 t0 = __builtin_amdgcn_ds_read_tr16_b64_v4i16((LAS bf16x4*)a0);
  bf16x4 t1 = __builtin_amdgcn_ds_read_tr16_b64_v4i16((LAS bf16x4*)a1);
  return __builtin_shufflevector(t0,t1,0,1,2,3,4,5,6,7);
}
DEV bf16x8 ldsb128(u32 a){ return *(LAS bf16x8*)a; }
DEV f32x4 mfma32(bf16x8 a, bf16x8 b, f32x4 c){ return __builtin_amdgcn_mfma_f32_16x16x32_bf16(a,b,c,0,0,0); }
DEV f32x4 mfma16(bf16x4 a, bf16x4 b, f32x4 c){ return __builtin_amdgcn_mfma_f32_16x16x16bf16_1k(a,b,c,0,0,0); }
DEV u32 pk2(float a, float b){ bf2v v = {(__bf16)a,(__bf16)b}; return __builtin_bit_cast(u32,v); }
DEV float lo2f(u32 u){ return __builtin_bit_cast(float, u<<16); }
DEV float hi2f(u32 u){ return __builtin_bit_cast(float, u&0xffff0000u); }
DEV bf16x4 pk4(float a,float b,float c,float d){ u32x2 v={pk2(a,b),pk2(c,d)}; return __builtin_bit_cast(bf16x4,v); }
DEV float xsum(float v){ v += __shfl_xor(v,16); v += __shfl_xor(v,32); return v; }
DEV float xmax(float v){ v = fmaxf(v,__shfl_xor(v,16)); v = fmaxf(v,__shfl_xor(v,32)); return v; }

constexpr int K1_STAGE=34816, K1_ONES=4*K1_STAGE, K1_PART=K1_ONES+256, K1_STAT=K1_PART+2560, K1_CS=K1_STAT+640, K1_LDS=K1_CS+768;
__global__ __launch_bounds__(512,2) void k1(const u16* __restrict__ x, const u16* __restrict__ wqkv, char* __restrict__ ws){
  extern __shared__ __attribute__((aligned(16))) char smem[];
  const u32 sb = (u32)(size_t)(LAS char*)smem;
  const int tid=threadIdx.x, l=tid&63, w=__builtin_amdgcn_readfirstlane(tid>>6);
  const int head=w&3, mh=w>>2, g=l>>4, m=l&15;
  const int bid=blockIdx.x, xcd=bid&7, idx=bid>>3;
  const int hg=4*(xcd&1)+(idx&3), rp=8*(xcd>>1)+(idx>>2), r0=rp*64;
  const int rot=(idx>>2)&7;
  const rsrc_t rsX=mkrsrc(x,S*D*2), rsW=mkrsrc(wqkv,D*3*D*2);
  const u32 voffW = (l>>3)*3072 + (((l&7) ^ (((((l>>4)&1)<<1)|(w&1))<<1))*16);
  u32 voffX0, voffX1;
  { int row=8*w+(l>>3); int gr=max(r0-16+row,0); voffX0 = gr*1024 + (((l&7)^((row>>1)&7))*16);
    row=64+8*(w&1)+(l>>3); gr=r0-16+row; voffX1 = gr*1024 + (((l&7)^((row>>1)&7))*16); }
  auto issue=[&](int i){
    const int st=(i+rot)&7; const u32 slot=sb+(i&3)*K1_STAGE;
    #pragma unroll
    for(int p=0;p<3;p++) dma(rsW, voffW, (st*64+8*w)*3072 + p*1024 + hg*128, slot+10240+p*8192+w*1024);
    dma(rsX, voffX0, st*128, slot+w*1024);
    if(w<2) dma(rsX, voffX1, st*128, slot+8192+w*1024);
  };
  issue(0); issue(1); issue(2); issue(3);
  if(tid<64) *(LAS u32*)(sb+K1_ONES+tid*4)=0x3f803f80u;
  if(bid==0 && tid<256) ((u32*)(ws+WS_FLAGS))[tid*32]=0u;
  const u32 xo0 = (u32)((32*mh+m)*128 + ((g ^ (m>>1))*16));
  const u32 xo1 = (u32)((32*mh+m)*128 + (((4+g) ^ (m>>1))*16));
  const bool onesl = (mh==0) && (m==0);
  const int q4=(l&15)>>2, p4=l&3;
  const u32 wo = (u32)(10240 + (8*g+q4)*128 + ((((head ^ ((((q4>>1)&1)<<1)|(g&1)))<<1)|(p4>>1))*16) + (p4&1)*8);
  f32x4 aq[3], ak[3], av[3];
  #pragma unroll
  for(int t=0;t<3;t++){ aq[t]=f32x4{0,0,0,0}; ak[t]=aq[t]; av[t]=aq[t]; }
  float ssum[3]={0,0,0}, ssq[3]={0,0,0};
  #pragma unroll
  for(int i=0;i<8;i++){
    if(i==0) waitv<12>(); else if(i<6) waitv<8>(); else if(i==6) waitv<4>(); else waitv<0>();
    BAR();
    if(i>=1 && i+3<8) issue(i+3);
    const u32 slot=sb+(i&3)*K1_STAGE;
    const int st=(i+rot)&7;
    #pragma unroll
    for(int ksl=0;ksl<2;ksl++){
      const u32 xa = slot + (ksl?xo1:xo0);
      bf16x8 xb[3];
      xb[0]=ldsb128(onesl ? sb+K1_ONES : xa);
      xb[1]=ldsb128(xa+2048); xb[2]=ldsb128(xa+4096);
      const u32 wa = slot + wo + ksl*4096;
      bf16x8 wq=trfrag(wa,wa+512), wk=trfrag(wa+8192,wa+8192+512), wv=trfrag(wa+16384,wa+16384+512);
      #pragma unroll
      for(int t=0;t<3;t++){
        aq[t]=mfma32(wq,xb[t],aq[t]); ak[t]=mfma32(wk,xb[t],ak[t]); av[t]=mfma32(xb[t],wv,av[t]);
      }
      if((st&3)==head){
        #pragma unroll
        for(int t=0;t<3;t++){ u32x4 u=__builtin_bit_cast(u32x4,xb[t]);
          #pragma unroll
          for(int e=0;e<4;e++){ float a=lo2f(u[e]), b=hi2f(u[e]); ssum[t]+=a; ssum[t]+=b; ssq[t]=fmaf(a,a,ssq[t]); ssq[t]=fmaf(b,b,ssq[t]); } }
      }
    }
  }
  #pragma unroll
  for(int t=0;t<3;t++){ float s=xsum(ssum[t]), qq=xsum(ssq[t]);
    if(g==0 && !(mh==0 && t==2)) *(LAS f32x2*)(sb+K1_PART+((head*5+2*mh+t)*16+m)*8)=f32x2{s,qq}; }
  if(mh==0){
    if(m==0){ *(LAS f32x4*)(sb+K1_CS+(head*3+0)*64+g*16)=aq[0]; *(LAS f32x4*)(sb+K1_CS+(head*3+1)*64+g*16)=ak[0]; }
    if(g==0) *(LAS float*)(sb+K1_CS+(head*3+2)*64+m*4)=av[0][0];
  }
  BAR();
  if(tid<80){ float s=0,qq=0;
    #pragma unroll
    for(int h=0;h<4;h++){ f32x2 v=*(LAS f32x2*)(sb+K1_PART+(h*80+tid)*8); s+=v[0]; qq+=v[1]; }
    float mean=s*(1.f/D); float var=fmaxf(qq*(1.f/D)-mean*mean,0.f);
    *(LAS f32x2*)(sb+K1_STAT+tid*8)=f32x2{mean, rsqrtf(var+EPS)}; }
  BAR();
  const f32x4 csq=*(LAS f32x4*)(sb+K1_CS+(head*3+0)*64+g*16), csk=*(LAS f32x4*)(sb+K1_CS+(head*3+1)*64+g*16);
  const float csv=*(LAS float*)(sb+K1_CS+(head*3+2)*64+m*4);
  bf16x4 qb[3], kb[3], vb[3];
  #pragma unroll
  for(int t=0;t<3;t++){
    const int rb=(2*mh+t)*16;
    f32x2 st=*(LAS f32x2*)(sb+K1_STAT+(rb+m)*8);
    qb[t]=pk4(st[1]*(aq[t][0]-st[0]*csq[0]), st[1]*(aq[t][1]-st[0]*csq[1]), st[1]*(aq[t][2]-st[0]*csq[2]), st[1]*(aq[t][3]-st[0]*csq[3]));
    kb[t]=pk4(st[1]*(ak[t][0]-st[0]*csk[0]), st[1]*(ak[t][1]-st[0]*csk[1]), st[1]*(ak[t][2]-st[0]*csk[2]), st[1]*(ak[t][3]-st[0]*csk[3]));
    f32x4 s01=*(LAS f32x4*)(sb+K1_STAT+(rb+4*g)*8), s23=*(LAS f32x4*)(sb+K1_STAT+(rb+4*g+2)*8);
    vb[t]=pk4(s01[1]*(av[t][0]-s01[0]*csv), s01[3]*(av[t][1]-s01[2]*csv), s23[1]*(av[t][2]-s23[0]*csv), s23[3]*(av[t][3]-s23[2]*csv));
  }
  const int par=l&1;
  const int hglob=hg*4+head;
  #pragma unroll
  for(int t=1;t<3;t++){
    const f32x4 z={0,0,0,0};
    f32x4 sc=mfma16(kb[t],qb[t],z), sp=mfma16(kb[t-1],qb[t],z);
    const bool noprev = (r0==0) && (mh==0) && (t==1);
    const bool c0 = (4*g+par) <= m, c1 = (4*g+par+2) <= m;
    float s0 = c0 ? (par?sc[1]:sc[0]) : (noprev ? -INFINITY : (par?sp[1]:sp[0]));
    float s1 = c1 ? (par?sc[3]:sc[2]) : (noprev ? -INFINITY : (par?sp[3]:sp[2]));
    const float mx=xmax(fmaxf(s0,s1));
    const float cexp=0.25f*1.44269504f;
    float p0=__builtin_amdgcn_exp2f((s0-mx)*cexp), p1=__builtin_amdgcn_exp2f((s1-mx)*cexp);
    const float inv=__builtin_amdgcn_rcpf(xsum(p0+p1));
    p0*=inv; p1*=inv;
    float pp0=c0?0.f:p0, pc0=c0?p0:0.f, pp1=c1?0.f:p1, pc1=c1?p1:0.f;
    u32x4 pb;
    pb[0]= par? pk2(0.f,pp0):pk2(pp0,0.f); pb[1]= par? pk2(0.f,pp1):pk2(pp1,0.f);
    pb[2]= par? pk2(0.f,pc0):pk2(pc0,0.f); pb[3]= par? pk2(0.f,pc1):pk2(pc1,0.f);
    bf16x8 va=__builtin_shufflevector(vb[t-1],vb[t],0,1,2,3,4,5,6,7);
    f32x4 o=mfma32(va,__builtin_bit_cast(bf16x8,pb),z);
    const int mt=rp*4+2*mh+t-1;
    *(u32x2*)(ws+WS_O+((size_t)(mt*16+(hglob>>1))*1024)+l*16+(hglob&1)*8)=u32x2{pk2(o[0],o[1]),pk2(o[2],o[3])};
  }
}

DEV u32 wdma_voff(int l, int ldb){ int r=l>>3; int sw=(((r>>1)&1)<<1)|((r>>2)&1); return r*ldb + (((l&7)^(sw<<1))*16); }
DEV u32 wtr_off(int l, int nt){ int g=l>>4,q=(l&15)>>2,p=l&3; int sw=(((q>>1)&1)<<1)|(g&1); return (4*g+q)*128 + ((((nt^sw)<<1)|(p>>1))*16) + (p&1)*8; }
DEV void grp_map(int bid, int& p, int& j){ int xcd=bid&7, idx=bid>>3; p = (xcd>>1)*4 + (idx>>3); j = (xcd&1)*8 + (idx&7); }

__global__ __launch_bounds__(512,2) void k2a(const u16* __restrict__ x, const u16* __restrict__ wo, char* __restrict__ ws){
  extern __shared__ __attribute__((aligned(16))) char smem[];
  const u32 sb = (u32)(size_t)(LAS char*)smem;
  const int tid=threadIdx.x, l=tid&63, w=__builtin_amdgcn_readfirstlane(tid>>6), g=l>>4, m=l&15;
  int p,j; grp_map(blockIdx.x,p,j);
  const int cb=j&7, rb=j>>3, kg=w>>1, nh=w&1;
  const rsrc_t rsX=mkrsrc(x,S*D*2), rsW=mkrsrc(wo,D*D*2), rsA=mkrsrc(ws+WS_O,S*D*2);
  const u32 vW=wdma_voff(l,1024);
  #pragma unroll
  for(int i=0;i<8;i++) dma(rsW, vW, (64*w+8*i)*1024+cb*128, sb+(8*w+i)*1024);
  #pragma unroll
  for(int i=0;i<8;i++){ int ks=2*w+(i>>2), a=i&3; int mt=8*p+2*rb+(a&1)+4*(a>>1);
    dma(rsA, l*16, (mt*16+ks)*1024, sb+65536+(ks*4+a)*1024); }
  { int row=8*w+(l>>3); int a=row>>4; int grow=(8*p+2*rb+(a&1)+4*(a>>1))*16+(row&15);
    dma(rsX, grow*1024+(((l&7)^((row>>1)&7))*16), cb*128, sb+131072+w*1024); }
  f32x4 acc[4][2];
  #pragma unroll
  for(int a=0;a<4;a++){ acc[a][0]=f32x4{0,0,0,0}; acc[a][1]=acc[a][0]; }
  const u32 w0=wtr_off(l,2*nh), w1=wtr_off(l,2*nh+1);
  WAITV(0); BAR();
  #pragma unroll
  for(int i=0;i<4;i++){ const int ks=4*kg+i;
    bf16x8 f0=trfrag(sb+ks*4096+w0, sb+ks*4096+2048+w0), f1=trfrag(sb+ks*4096+w1, sb+ks*4096+2048+w1);
    #pragma unroll
    for(int a=0;a<4;a++){ bf16x8 b=ldsb128(sb+65536+(ks*4+a)*1024+l*16); acc[a][0]=mfma32(f0,b,acc[a][0]); acc[a][1]=mfma32(f1,b,acc[a][1]); }
  }
  BAR();
  #pragma unroll
  for(int a=0;a<4;a++) if(a!=kg){
    *(LAS f32x4*)(sb+(((kg*4+a)*2+nh)*2+0)*1024+l*16)=acc[a][0];
    *(LAS f32x4*)(sb+(((kg*4+a)*2+nh)*2+1)*1024+l*16)=acc[a][1]; }
  BAR();
  f32x4 r0v,r1v;
  { r0v = kg==0?acc[0][0]:kg==1?acc[1][0]:kg==2?acc[2][0]:acc[3][0];
    r1v = kg==0?acc[0][1]:kg==1?acc[1][1]:kg==2?acc[2][1]:acc[3][1]; }
  #pragma unroll
  for(int s=0;s<4;s++) if(s!=kg){
    r0v+=*(LAS f32x4*)(sb+(((s*4+kg)*2+nh)*2+0)*1024+l*16);
    r1v+=*(LAS f32x4*)(sb+(((s*4+kg)*2+nh)*2+1)*1024+l*16); }
  const int row=16*kg+m;
  u32x2 x0=*(LAS u32x2*)(sb+131072+row*128+(((4*nh+(g>>1))^((row>>1)&7))*16)+(g&1)*8);
  u32x2 x1=*(LAS u32x2*)(sb+131072+row*128+(((4*nh+2+(g>>1))^((row>>1)&7))*16)+(g&1)*8);
  u32x4 hv;
  hv[0]=pk2(r0v[0]+lo2f(x0[0]), r0v[1]+hi2f(x0[0])); hv[1]=pk2(r0v[2]+lo2f(x0[1]), r0v[3]+hi2f(x0[1]));
  hv[2]=pk2(r1v[0]+lo2f(x1[0]), r1v[1]+hi2f(x1[0])); hv[3]=pk2(r1v[2]+lo2f(x1[1]), r1v[3]+hi2f(x1[1]));
  const int mt=8*p+2*rb+(kg&1)+4*(kg>>1);
  *(u32x4*)(ws+WS_H+(size_t)(mt*16+cb*2+nh)*1024+l*16)=hv;
  float s=0,qq=0;
  #pragma unroll
  for(int e=0;e<4;e++){ float a=lo2f(hv[e]), b=hi2f(hv[e]); s+=a+b; qq=fmaf(a,a,qq); qq=fmaf(b,b,qq); }
  s=xsum(s); qq=xsum(qq);
  if(g==0) *(f32x2*)(ws+WS_ST+((size_t)(mt*16+m)*16+cb*2+nh)*8)=f32x2{s,qq};
}

constexpr int B_STAGE=24576, B_NST=6, B_STAT=B_NST*B_STAGE, B_LDS=B_STAT+1024;
__global__ __launch_bounds__(512,2) void k2b(const u16* __restrict__ wg, const u16* __restrict__ wu, char* __restrict__ ws){
  extern __shared__ __attribute__((aligned(16))) char smem[];
  const u32 sb = (u32)(size_t)(LAS char*)smem;
  const int tid=threadIdx.x, l=tid&63, w=__builtin_amdgcn_readfirstlane(tid>>6), g=l>>4, m=l&15;
  int p,j; grp_map(blockIdx.x,p,j);
  if(j>=14) return;
  const rsrc_t rsG=mkrsrc(wg,D*DF*2), rsU=mkrsrc(wu,D*DF*2), rsA=mkrsrc(ws+WS_H,S*D*2);
  const u32 vW=wdma_voff(l,DF*2);
  const rsrc_t rsWm = (w<4)? rsG : rsU;
  auto issue=[&](int ks){
    const u32 slot=sb+(ks%B_NST)*B_STAGE;
    #pragma unroll
    for(int i=0;i<2;i++) dma(rsWm, vW, (32*ks+16*(w&1)+8*i)*(DF*2) + j*256 + ((w>>1)&1)*128, slot+(w>>1)*4096+(2*(w&1)+i)*1024);
    dma(rsA, l*16, ((8*p+w)*16+ks)*1024, slot+16384+w*1024);
  };
  #pragma unroll
  for(int i=0;i<B_NST;i++) issue(i);
  if(tid<128){ const f32x4* sp=(const f32x4*)(ws+WS_ST+(size_t)(p*128+tid)*128); float s=0,qq=0;
    #pragma unroll
    for(int i=0;i<8;i++){ f32x4 v=sp[i]; s+=v[0]+v[2]; qq+=v[1]+v[3]; }
    float mean=s*(1.f/D); float var=fmaxf(qq*(1.f/D)-mean*mean,0.f);
    *(LAS f32x2*)(sb+B_STAT+tid*8)=f32x2{mean,rsqrtf(var+EPS)}; }
  f32x4 ag[8], au[8], cg={0,0,0,0}, cu={0,0,0,0};
  #pragma unroll
  for(int a=0;a<8;a++){ ag[a]=f32x4{0,0,0,0}; au[a]=ag[a]; }
  const u32 wo=wtr_off(l,w&3)+(w>>2)*4096;
  const u32x4 onesu={0x3f803f80u,0x3f803f80u,0x3f803f80u,0x3f803f80u};
  const bf16x8 ones=__builtin_bit_cast(bf16x8,onesu);
  #pragma unroll
  for(int ks=0;ks<16;ks++){
    if(ks==0) waitv<15>(); else if(ks<=11) waitv<12>(); else if(ks==12) waitv<9>(); else if(ks==13) waitv<6>(); else if(ks==14) waitv<3>(); else waitv<0>();
    BAR();
    if(ks>=1 && ks+B_NST-1<16) issue(ks+B_NST-1);
    const u32 slot=sb+(ks%B_NST)*B_STAGE;
    bf16x8 fg=trfrag(slot+wo,slot+wo+2048), fu=trfrag(slot+8192+wo,slot+8192+wo+2048);
    cg=mfma32(fg,ones,cg); cu=mfma32(fu,ones,cu);
    #pragma unroll
    for(int a=0;a<8;a++){ bf16x8 b=ldsb128(slot+16384+a*1024+l*16); ag[a]=mfma32(fg,b,ag[a]); au[a]=mfma32(fu,b,au[a]); }
  }
  #pragma unroll
  for(int a=0;a<8;a++){
    f32x2 st=*(LAS f32x2*)(sb+B_STAT+(a*16+m)*8);
    float hv[4];
    #pragma unroll
    for(int r=0;r<4;r++){
      float gg=st[1]*(ag[a][r]-st[0]*cg[r]), uu=st[1]*(au[a][r]-st[0]*cu[r]);
      float t=gg*gg; float pz=fmaf(t,-0.1029432397f,-2.302208198f);
      float e=__builtin_amdgcn_exp2f(gg*pz);
      hv[r]=gg*uu*__builtin_amdgcn_rcpf(1.f+e);
    }
    *(u32x2*)(ws+WS_HH+(size_t)((8*p+a)*56+j*4+(w>>1))*1024+l*16+(w&1)*8)=u32x2{pk2(hv[0],hv[1]),pk2(hv[2],hv[3])};
  }
}

constexpr int C_STAGE=32768, C_NST=5, C_LDS=C_NST*C_STAGE;
__global__ __launch_bounds__(512,2) void k2c(const u16* __restrict__ wd, char* __restrict__ ws, u16* __restrict__ out){
  extern __shared__ __attribute__((aligned(16))) char smem[];
  const u32 sb = (u32)(size_t)(LAS char*)smem;
  const int tid=threadIdx.x, l=tid&63, w=__builtin_amdgcn_readfirstlane(tid>>6), g=l>>4, m=l&15;
  int p,j; grp_map(blockIdx.x,p,j);
  const int cb=j&7, rb=j>>3, kg=w>>1, nh=w&1;
  const rsrc_t rsW=mkrsrc(wd,DF*D*2), rsA=mkrsrc(ws+WS_HH,S*DF*2);
  const u32 vW=wdma_voff(l,1024);
  auto issue=[&](int s){
    const u32 slot=sb+(s%C_NST)*C_STAGE+kg*8192; const int ks=14*kg+s;
    #pragma unroll
    for(int i=0;i<2;i++) dma(rsW, vW, (32*ks+16*nh+8*i)*1024+cb*128, slot+(2*nh+i)*1024);
    #pragma unroll
    for(int i=0;i<2;i++){ int mt=8*p+2*rb+i+4*nh; dma(rsA, l*16, (mt*56+ks)*1024, slot+4096+(2*nh+i)*1024); }
  };
  #pragma unroll
  for(int i=0;i<C_NST;i++) issue(i);
  f32x4 acc[4][2];
  #pragma unroll
  for(int a=0;a<4;a++){ acc[a][0]=f32x4{0,0,0,0}; acc[a][1]=acc[a][0]; }
  const u32 w0=wtr_off(l,2*nh), w1=wtr_off(l,2*nh+1);
  #pragma unroll
  for(int s=0;s<14;s++){
    if(s==0) waitv<16>(); else if(s<=10) waitv<12>(); else if(s==11) waitv<8>(); else if(s==12) waitv<4>(); else waitv<0>();
    BAR();
    if(s>=1 && s+C_NST-1<14) issue(s+C_NST-1);
    const u32 slot=sb+(s%C_NST)*C_STAGE+kg*8192;
    bf16x8 f0=trfrag(slot+w0,slot+2048+w0), f1=trfrag(slot+w1,slot+2048+w1);
    #pragma unroll
    for(int a=0;a<4;a++){ bf16x8 b=ldsb128(slot+4096+a*1024+l*16); acc[a][0]=mfma32(f0,b,acc[a][0]); acc[a][1]=mfma32(f1,b,acc[a][1]); }
  }
  const int mt=8*p+2*rb+(kg&1)+4*(kg>>1);
  u32x4 hv=*(const u32x4*)(ws+WS_H+(size_t)(mt*16+cb*2+nh)*1024+l*16);
  BAR();
  #pragma unroll
  for(int a=0;a<4;a++) if(a!=kg){
    *(LAS f32x4*)(sb+(((kg*4+a)*2+nh)*2+0)*1024+l*16)=acc[a][0];
    *(LAS f32x4*)(sb+(((kg*4+a)*2+nh)*2+1)*1024+l*16)=acc[a][1]; }
  BAR();
  f32x4 r0v,r1v;
  { r0v = kg==0?acc[0][0]:kg==1?acc[1][0]:kg==2?acc[2][0]:acc[3][0];
    r1v = kg==0?acc[0][1]:kg==1?acc[1][1]:kg==2?acc[2][1]:acc[3][1]; }
  #pragma unroll
  for(int s=0;s<4;s++) if(s!=kg){
    r0v+=*(LAS f32x4*)(sb+(((s*4+kg)*2+nh)*2+0)*1024+l*16);
    r1v+=*(LAS f32x4*)(sb+(((s*4+kg)*2+nh)*2+1)*1024+l*16); }
  u16* op=out+(size_t)(mt*16+m)*D+cb*64+nh*32+4*g;
  *(u32x2*)op      =u32x2{pk2(r0v[0]+lo2f(hv[0]),r0v[1]+hi2f(hv[0])), pk2(r0v[2]+lo2f(hv[1]),r0v[3]+hi2f(hv[1]))};
  *(u32x2*)(op+16) =u32x2{pk2(r1v[0]+lo2f(hv[2]),r1v[1]+hi2f(hv[2])), pk2(r1v[2]+lo2f(hv[3]),r1v[3]+hi2f(hv[3]))};
}

extern "C" void kernel_launch(void* const* d_in, const int* in_sizes, int n_in, void* d_out, int out_size, void* d_ws, size_t ws_size, hipStream_t stream){
  const u16* x=(const u16*)d_in[0]; const u16* wqkv=(const u16*)d_in[1]; const u16* wo=(const u16*)d_in[2];
  const u16* wg=(const u16*)d_in[4]; const u16* wu=(const u16*)d_in[5]; const u16* wd=(const u16*)d_in[6];
  char* ws=(char*)d_ws;
  hipFuncSetAttribute((const void*)k1, hipFuncAttributeMaxDynamicSharedMemorySize, K1_LDS);
  hipFuncSetAttribute((const void*)k2a, hipFuncAttributeMaxDynamicSharedMemorySize, 139264);
  hipFuncSetAttribute((const void*)k2b, hipFuncAttributeMaxDynamicSharedMemorySize, B_LDS);
  hipFuncSetAttribute((const void*)k2c, hipFuncAttributeMaxDynamicSharedMemorySize, C_LDS);
  k1<<<256,512,K1_LDS,stream>>>(x,wqkv,ws);
  k2a<<<256,512,139264,stream>>>(x,wo,ws);
  k2b<<<256,512,B_LDS,stream>>>(wg,wu,ws);
  k2c<<<256,512,C_LDS,stream>>>(wd,ws,(u16*)d_out);
}
```

```cpp
#define STMOD "sc1"
#define LDMOD "sc1"
#define ST16O ST16
#define STL 1
#define FLO 0
#define IPF 2
#define IDL 3
#define MAP 0
#define R2 0
#define R4 0
#define AV3 0
#define LOC2 1
#define LOCH 1
#define LOC4 1
#define IDXR 0
#define XHX 0
#define XCB 0
#define FLB 256
#define SWO 0
#define SWH 0
#define SWHH 0

#include <hip/hip_runtime.h>
#include <hip/hip_bf16.h>
#define LAS __attribute__((address_space(3)))
typedef unsigned short u16; typedef unsigned u32; typedef unsigned long long u64;
typedef short bf16x8 __attribute__((ext_vector_type(8)));
typedef short bf16x4 __attribute__((ext_vector_type(4)));
typedef float f32x4 __attribute__((ext_vector_type(4)));
typedef float f32x2 __attribute__((ext_vector_type(2)));
typedef float f32x16 __attribute__((ext_vector_type(16)));
typedef unsigned u32x4 __attribute__((ext_vector_type(4)));
typedef unsigned u32x2 __attribute__((ext_vector_type(2)));
typedef __bf16 bf2v __attribute__((ext_vector_type(2)));
typedef __amdgpu_buffer_rsrc_t rsrc_t;
#define DEV __device__ __forceinline__

constexpr int S=2048, D=512, DF=1792;
constexpr float EPS=1e-5f;
constexpr u32 WS_FLAGS=0;
constexpr u32 WS_O  = 65536;
constexpr u32 WS_H  = WS_O + S*D*2;
constexpr u32 WS_ST = WS_H + S*D*2;
constexpr u32 WS_HH = WS_ST + 16*8*128*16;
constexpr u32 WS_END= WS_HH + S*DF*2;
#define HH_FT16 1
constexpr u32 WS_MIR = (16u<<20)+128u;
#define FX(v) ((v)+((AV3 && ((((v)+wsl)>>7)&7u)==3u)?WS_MIR:0u))

__shared__ __attribute__((aligned(16))) char smem[163840];

DEV rsrc_t mkrsrc(const void* p, u32 bytes){ return __builtin_amdgcn_make_buffer_rsrc((void*)p, 0, bytes, 0x00020000); }
#define DMA(rs,voff,sbase,gc,wk,lds) do{ u32 t_; asm volatile("s_add_u32 m0, %5, %6\n\ts_add_u32 %0, %3, %4\n\tbuffer_load_dwordx4 %1, %2, %0 offen lds" : "=&s"(t_) : "v"(voff), "s"(rs), "s"(sbase), "i"(gc), "s"(wk), "i"(lds) : "memory","scc","m0"); }while(0)
#define LD16(dst,rs,voff,soff,imm) asm volatile("buffer_load_dwordx4 %0, %1, %2, %3 offen offset:" #imm " " LDMOD : "=v"(dst) : "v"(voff), "s"(rs), "s"(soff) : "memory")
#define LD8(dst,rs,voff,soff,imm)  asm volatile("buffer_load_dwordx2 %0, %1, %2, %3 offen offset:" #imm " sc1" : "=v"(dst) : "v"(voff), "s"(rs), "s"(soff) : "memory")
#define LD8P(dst,rs,voff,soff,imm) asm volatile("buffer_load_dwordx2 %0, %1, %2, %3 offen offset:" #imm : "=v"(dst) : "v"(voff), "s"(rs), "s"(soff) : "memory")
#define ST16(src,rs,voff,soff) asm volatile("buffer_store_dwordx4 %0, %1, %2, %3 offen " STMOD "\n\ts_nop 1" :: "v"(src), "v"(voff), "s"(rs), "s"(soff) : "memory")
#define ST16P(src,rs,voff,soff) asm volatile("buffer_store_dwordx4 %0, %1, %2, %3 offen\n\ts_nop 1" :: "v"(src), "v"(voff), "s"(rs), "s"(soff) : "memory")
#define ST8(src,rs,voff,soff)  asm volatile("buffer_store_dwordx2 %0, %1, %2, %3 offen sc1" :: "v"(src), "v"(voff), "s"(rs), "s"(soff) : "memory")
#define FLAG(sel,soff) do{ u64 t_; asm volatile("v_mov_b64 %0, %4\n\ts_cmp_eq_u32 %1, 0\n\ts_cselect_b64 exec, 1, 0\n\tbuffer_store_dwordx2 %0, off, %2, %3 sc1\n\ts_mov_b64 exec, -1" : "=&v"(t_) : "s"(sel), "s"(rsS), "s"(soff), "s"(tok) : "memory","scc"); }while(0)
#define FLAGW(soff) asm volatile("s_mov_b64 exec, 1\n\tbuffer_store_dwordx2 %0, off, %1, %2 sc1\n\ts_mov_b64 exec, -1" :: "v"(tokv), "s"(rsS), "s"(soff) : "memory")
#define TCHG(d,a) asm volatile("global_load_dword %0, %1, off" : "=v"(d) : "v"(a) : "memory")
#define TCHB(d,rs,voff,soff) asm volatile("buffer_load_dword %0, %1, %2, %3 offen" : "=v"(d) : "v"(voff), "s"(rs), "s"((u32)soff) : "memory")
#define TCHS(d,rs,voff,soff) asm volatile("buffer_load_dword %0, %1, %2, %3 offen sc1" : "=v"(d) : "v"(voff), "s"(rs), "s"(soff) : "memory")
#define CHKTAIL(b) "\n\ts_cbranch_vccz 2f\n\ts_cmp_eq_u32 " b ", 0\n\ts_cbranch_scc1 2f\n\ts_sub_u32 " b ", " b ", 1\n\t"
#define CHK1(N,A,voff,soff,i0) do{ asm volatile("s_waitcnt vmcnt(" #N ")\n" \
 "1:\n\tv_max3_u32 %1, %3, %4, %5\n\tv_max_u32 %1, %1, %6\n\tv_cmp_eq_u32 vcc, -1, %1" CHKTAIL("%2") \
 "buffer_load_dwordx4 %0, %7, %8, %9 offen offset:" #i0 " sc1\n\ts_waitcnt vmcnt(0)\n\ts_branch 1b\n2:" \
 : "+v"(A), "+v"(ctmp), "+s"(bud) : "v"(A[0]),"v"(A[1]),"v"(A[2]),"v"(A[3]), "v"(voff), "s"(rsS), "s"(soff) : "memory","vcc","scc"); }while(0)
#define CHK2(N,A,B,voff,s0,i0,s1,i1) do{ asm volatile("s_waitcnt vmcnt(" #N ")\n" \
 "1:\n\tv_max3_u32 %2, %4, %5, %6\n\tv_max3_u32 %2, %2, %7, %8\n\tv_max3_u32 %2, %2, %9, %10\n\tv_max_u32 %2, %2, %11\n\tv_cmp_eq_u32 vcc, -1, %2" CHKTAIL("%3") \
 "buffer_load_dwordx4 %0, %12, %13, %14 offen offset:" #i0 " sc1\n\tbuffer_load_dwordx4 %1, %12, %13, %15 offen offset:" #i1 " sc1\n\ts_waitcnt vmcnt(0)\n\ts_branch 1b\n2:" \
 : "+v"(A), "+v"(B), "+v"(ctmp), "+s"(bud) : "v"(A[0]),"v"(A[1]),"v"(A[2]),"v"(A[3]),"v"(B[0]),"v"(B[1]),"v"(B[2]),"v"(B[3]), "v"(voff), "s"(rsS), "s"(s0), "s"(s1) : "memory","vcc","scc"); }while(0)
#define LDR(d,s,i) "buffer_load_dwordx4 " d ", %[vo], %[rs], " s " offen offset:" #i " sc1\n\t"
#define CHKHEAD(N) "s_waitcnt vmcnt(" #N ")\n1:\n\tv_max3_u32 %[t], %[a0], %[a1], %[a2]\n\tv_max3_u32 %[t], %[t], %[a3], %[b0]\n\tv_max3_u32 %[t], %[t], %[b1], %[b2]\n\tv_max_u32 %[t], %[t], %[b3]\n\tv_cmp_eq_u32 vcc, -1, %[t]" CHKTAIL("%[bd]") "s_sleep 1\n\t"
#define CHKIN(A,B) [a0]"v"(A[0]),[a1]"v"(A[1]),[a2]"v"(A[2]),[a3]"v"(A[3]),[b0]"v"(B[0]),[b1]"v"(B[1]),[b2]"v"(B[2]),[b3]"v"(B[3]), [vo]"v"(g16), [rs]"s"(rsS)
#define CHKEND "s_waitcnt vmcnt(0)\n\ts_branch 1b\n2:"
#define CHKR2(N,A,B,C,D,S0_,S1_) asm volatile(CHKHEAD(N) LDR("%0","%[s0]",0) LDR("%1","%[s0]",1024) LDR("%2","%[s1]",0) LDR("%3","%[s1]",1024) CHKEND \
 : "+v"(A),"+v"(B),"+v"(C),"+v"(D),[t]"+v"(ctmp),[bd]"+s"(bud) : CHKIN(A,B), [s0]"s"(S0_),[s1]"s"(S1_) : "memory","vcc","scc")
#define CHKR3(N,A,B,C,D,E,F,S0_,S1_,S2_) asm volatile(CHKHEAD(N) LDR("%0","%[s0]",0) LDR("%1","%[s0]",1024) LDR("%2","%[s1]",0) LDR("%3","%[s1]",1024) LDR("%4","%[s2]",0) LDR("%5","%[s2]",1024) CHKEND \
 : "+v"(A),"+v"(B),"+v"(C),"+v"(D),"+v"(E),"+v"(F),[t]"+v"(ctmp),[bd]"+s"(bud) : CHKIN(A,B), [s0]"s"(S0_),[s1]"s"(S1_),[s2]"s"(S2_) : "memory","vcc","scc")
#define CHKR4(N,A,B,C,D,E,F,G,H,S0_,S1_,S2_,S3_) asm volatile(CHKHEAD(N) LDR("%0","%[s0]",0) LDR("%1","%[s0]",1024) LDR("%2","%[s1]",0) LDR("%3","%[s1]",1024) LDR("%4","%[s2]",0) LDR("%5","%[s2]",1024) LDR("%6","%[s3]",0) LDR("%7","%[s3]",1024) CHKEND \
 : "+v"(A),"+v"(B),"+v"(C),"+v"(D),"+v"(E),"+v"(F),"+v"(G),"+v"(H),[t]"+v"(ctmp),[bd]"+s"(bud) : CHKIN(A,B), [s0]"s"(S0_),[s1]"s"(S1_),[s2]"s"(S2_),[s3]"s"(S3_) : "memory","vcc","scc")
#define CHKF(N,F,voff,soff) do{ asm volatile("s_waitcnt vmcnt(" #N ")\n" \
 "1:\n\tv_cmp_ne_u64 vcc, %5, %0" CHKTAIL("%1") \
 "s_sleep 1\n\tbuffer_load_dwordx2 %0, %2, %3, %4 offen sc1\n\ts_waitcnt vmcnt(0)\n\ts_branch 1b\n2:" \
 : "+v"(F), "+s"(bud) : "v"(voff), "s"(rsS), "s"(soff), "s"(tok) : "memory","vcc","scc"); }while(0)
#define CHKP(N,F,voff,soff) do{ asm volatile("s_waitcnt vmcnt(" #N ")\n" \
 "1:\n\tv_cmp_eq_u32 vcc, -1, %0" CHKTAIL("%1") \
 "s_sleep 1\n\tbuffer_load_dword %0, %2, %3, %4 offen sc1\n\ts_waitcnt vmcnt(0)\n\ts_branch 1b\n2:" \
 : "+v"(F), "+s"(bud) : "v"(voff), "s"(rsS), "s"(soff) : "memory","vcc","scc"); }while(0)
#define SB __builtin_amdgcn_sched_barrier(0)
#define RAWBAR() do{ SB; asm volatile("s_barrier" ::: "memory"); SB; }while(0)
#define BAR() do{ SB; __builtin_amdgcn_s_waitcnt(0xC07F); asm volatile("s_barrier" ::: "memory"); SB; }while(0)
#define OPQV(x) asm volatile("" : "+v"(x))
#define OPQS(x) asm volatile("" : "+s"(x))
#define VFMA(d,a,b,c) asm volatile("v_fma_f32 %0, %1, %2, %3" : "=v"(d) : "v"(a), "v"(b), "v"(c))
#define VMUL(d,a,b) asm volatile("v_mul_f32 %0, %1, %2" : "=v"(d) : "v"(a), "v"(b))
#define VEXP(d,a) asm volatile("v_exp_f32 %0, %1" : "=v"(d) : "v"(a))
#define VRCP(d,a) asm volatile("v_rcp_f32 %0, %1" : "=v"(d) : "v"(a))
#define VCVT(d,a,b) asm volatile("v_cvt_pk_bf16_f32 %0, %1, %2" : "=v"(d) : "v"(a), "v"(b))
DEV bf16x4 tr(u32 a){ return __builtin_amdgcn_ds_read_tr16_b64_v4i16((LAS bf16x4*)a); }
DEV bf16x8 cat(bf16x4 a, bf16x4 b){ return __builtin_shufflevector(a,b,0,1,2,3,4,5,6,7); }
DEV bf16x8 trfrag(u32 a0, u32 a1){ return cat(tr(a0),tr(a1)); }
DEV bf16x8 l128(u32 a){ return *(LAS bf16x8*)a; }
DEV f32x4 lf4(u32 a){ return *(LAS f32x4*)a; }
DEV f32x2 lf2(u32 a){ return *(LAS f32x2*)a; }
DEV float lf1(u32 a){ return *(LAS float*)a; }
DEV f32x4 mfma32(bf16x8 a, bf16x8 b, f32x4 c){ return __builtin_amdgcn_mfma_f32_16x16x32_bf16(a,b,c,0,0,0); }
DEV f32x4 mfma16(bf16x4 a, bf16x4 b, f32x4 c){ return __builtin_amdgcn_mfma_f32_16x16x16bf16_1k(a,b,c,0,0,0); }
DEV f32x16 mfmaL(bf16x8 a, bf16x8 b, f32x16 c){ return __builtin_amdgcn_mfma_f32_32x32x16_bf16(a,b,c,0,0,0); }
DEV bf16x8 asb(u32x4 v){ return __builtin_bit_cast(bf16x8,v); }
DEV u32x4 asq(bf16x8 v){ return __builtin_bit_cast(u32x4,v); }
DEV u32 pk2(float a, float b){ bf2v v = {(__bf16)a,(__bf16)b}; return __builtin_bit_cast(u32,v); }
DEV float asf(u32 u){ return __builtin_bit_cast(float,u); }
DEV u32 asu(float f){ return __builtin_bit_cast(u32,f); }
DEV float lo2f(u32 u){ return __builtin_bit_cast(float, u<<16); }
DEV float hi2f(u32 u){ return __builtin_bit_cast(float, u&0xffff0000u); }
DEV bf16x4 pk4(float a,float b,float c,float d){ u32x2 v={pk2(a,b),pk2(c,d)}; return __builtin_bit_cast(bf16x4,v); }
DEV float sel(bool c, float a, float b){ return c?a:b; }
DEV float fmx(float a, float b){ return __builtin_amdgcn_fmed3f(a,b,INFINITY); }
DEV float bc0(float v){ return asf((u32)__builtin_amdgcn_update_dpp(0,(int)asu(v),0x150,0xf,0xf,true)); }
DEV float row0(float v){ u32 u=asu(v); auto r=__builtin_amdgcn_permlane16_swap(u,u,false,false); u32 a=r[0];
  auto r2=__builtin_amdgcn_permlane32_swap(a,a,false,false); return asf(r2[0]); }
DEV float psum(float v){
  u32 u=asu(v);
  auto r=__builtin_amdgcn_permlane16_swap(u,u,false,false); v=asf(r[0])+asf(r[1]);
  u=asu(v);
  auto r2=__builtin_amdgcn_permlane32_swap(u,u,false,false); return asf(r2[0])+asf(r2[1]);
}
DEV float pmax(float v){
  u32 u=asu(v);
  auto r=__builtin_amdgcn_permlane16_swap(u,u,false,false); v=fmaxf(asf(r[0]),asf(r[1]));
  u=asu(v);
  auto r2=__builtin_amdgcn_permlane32_swap(u,u,false,false); return fmaxf(asf(r2[0]),asf(r2[1]));
}
#define STAMP(i)

__global__ __launch_bounds__(512,2) void fused(const u16* __restrict__ x, const u16* __restrict__ wqkv, const u16* __restrict__ wo,
    const u16* __restrict__ wg, const u16* __restrict__ wu, const u16* __restrict__ wd, char* ws, u16* out){
  u32 cpf;
  { u64 pc; u32 o_=(u32)((threadIdx.x&8)? 256+(((blockIdx.x>>3)&3)*8+(threadIdx.x>>6)) : ((((blockIdx.x>>3)&3)*8+(threadIdx.x>>6))*8+(threadIdx.x&7)))*128u;
    asm volatile("s_getpc_b64 %0\nfused_lo:\n\tv_min_u32 %2, fused_hi-fused_lo-4, %2\n\tglobal_load_dword %1, %2, %0" : "=&s"(pc), "=v"(cpf), "+v"(o_) :: "memory"); }

  const int tid=threadIdx.x, l=tid&63, w=__builtin_amdgcn_readfirstlane(tid>>6);
  const int bid=blockIdx.x, xcd=bid&7, tb=bid>>3;
  const int p=MAP? xcd*2+(tb>>4) : (xcd>>1)*4+(tb>>3), xh=MAP? (tb>>3)&1 : (xcd&1)^XHX, idx=(tb+IDXR)&7, rb=idx>>2, xc=xh^XCB, cb=xc*4+(idx&3);
  const int r0=p*128+rb*64;
  const int cb2=R2? idx : cb, rb2=R2? xh : rb, r02=p*128+rb2*64;
  const int cb4=R4? idx : cb, rb4=R4? xh : rb;
  const int rot=(p+idx)&3, rot1=(p+idx)&3;
  asm volatile("" :: "v"((LAS char*)smem));
  const rsrc_t rsX=mkrsrc(x,S*D*2), rsW=mkrsrc(wqkv,D*3*D*2);
  const u32 wK=w*1024, wK2=w*2048;
  asm volatile("; MARK start");
  STAMP(0);
  const u32 l3=l>>3, l7=l&7;
  const u32 sw1l=((((l>>4)&1)<<1)|(w&1));
  const u32 voffW=l3*3072+((l7^(sw1l<<1))*16);
  const u32 xsw=(l7^(((l3>>1)|((w&1)<<2))&7))*16;
  const u32 voffX=(r0+8*w+l3)*1024+xsw;
  const u32 voffH=max(r0-16+8*(w&1)+(int)l3,0)*1024+xsw;
  const u32 sHA=(((((w>>2)+rot1)&3)*2)+((w>>1)&1))*128, sHB=sHA^512;
  u32 sX[4], sW[4];
  _Pragma("unroll") for(int b=0;b<4;b++){ const u32 k_=(b+rot1)&3; sX[b]=k_*256; sW[b]=k_*393216+w*24576+cb*128; }
  const rsrc_t rsOut=mkrsrc(out,S*D*2);
  const int sWi=(p&3)*2+rb; u32 tc[17]; const u32 OOB=0x70000000u; const u32 uw=w*32+tb;
  DMA(rsX,voffH,sHA,0,wK,131072);
  DMA(rsX,voffX,sX[0],0,wK,0);
  DMA(rsW,voffW,sW[0],1024,wK,16384);
  DMA(rsW,voffW,sW[0],2048,wK,24576);
  DMA(rsW,voffW,sW[0],0,wK,8192);
  SB;
  const int g=l>>4, m=l&15, q4=(l&15)>>2, p4=l&3, hi=l>>5, nh=(l>>4)&1;
  const bool act3 = idx!=IDL;
  const int fb = xh*7 + (idx<IDL?idx:idx-1);
  const rsrc_t rsS=mkrsrc(ws,AV3?WS_MIR+WS_END:WS_END);
  const u32 l16=l*16; const u32 wsl=(u32)(u64)ws;
  u32 g16=FX(l16); OPQV(g16);
  const u32 fl=WS_FLAGS+p*4096;
  *(LAS u32*)(155648+(l&3)*4)=0x3f803f80u;
  u64 tok; asm volatile("s_load_dwordx2 %0, %1, 0x0" : "=s"(tok) : "s"(ws+fl) : "memory");
  u32 bud=2000000; u32 ctmp=l; OPQV(ctmp);
  const int head=w&3, mh=w>>2;
  const int ksA=head>>1; const u32 swz=(m>>1)&7;
  const u32 xA=m*128+(((4*ksA+g)^swz)*16), xB=m*128+(((4*(1-ksA)+g)^swz)*16);
  const bool onesl=(m==0);
  u32 a0A=onesl?155648:xA+(mh?2048:131072), a0B=onesl?155648:xB+(mh?2048:131072);
  u32 st0=onesl?0u:(mh?32768u:2048u), st4=onesl?0u:(mh?(u32)-98304:2048u);
  u32 vxA=xA+mh*4096, vxB=xB+mh*4096;
  const u32 sw1=(((q4>>1)&1)<<1)|(g&1);
  const u32 wbase=8192+(8*g+q4)*128+((head^sw1)*32)+p4*8;
  u32 wA=wbase+ksA*4096, wB=wbase+(1-ksA)*4096;
  OPQV(a0A); OPQV(a0B); OPQV(st0); OPQV(st4); OPQV(vxA); OPQV(vxB); OPQV(wA); OPQV(wB);
  SB;
  SB;
  const rsrc_t rsO=mkrsrc(wo,D*D*2), rsD=mkrsrc(wd,DF*D*2);
  const rsrc_t rsG=mkrsrc((w<4)?wg:wu,D*DF*2);
  const int mt2=w&3, kh=(w>>2)^(LOC2?(xc^(LOC2&1)):0);
  const int wh=w^(LOCH?4*(xc^(LOCH&1)):0), w4=w^(LOC4?4*(xh^(LOC4&1)):0);
  u32 yM=(u32)(head&1)-1u; OPQS(yM); u32 yO=(~yM)&0x3f803f80u; OPQV(yO);
  const u32 voffO=l3*1024+((l7^(sw1l<<1))*16);
  const bool pf4=(IPF==1) && !act3;
  const rsrc_t rsG3=mkrsrc(pf4?wd:((w<4)?wg:wu),D*DF*2);
  const u32 voff3=pf4? (l3>>2)*1024+xc*512+(l3&3)*128+l7*16 : (IPF==2 && !act3)? 0x70000000u : l3*3584+((l7^((((l>>4)&1)<<2)|((w>>2)<<1)))*16);
  u32 Bk[4]; _Pragma("unroll") for(int b=0;b<4;b++) Bk[b]=(b+rot)&3;
  u32 sO[4], s3[4];
  _Pragma("unroll") for(int b=0;b<4;b++){ sO[b]=(Bk[b]*64+8*w)*1024+cb2*128; s3[b]=(Bk[b]*128+16*(w&1))*3584+fb*256+((w>>1)&1)*128;
    if(pf4){ const int j_=min(b*8+w,27), sh_=j_>=14; s3[b]=((p&3)*448+sh_*56+(j_-14*sh_)*2)*1024; } }
  u32 vt3=((((l>>4)&1)*32)+((l>>1)&7))*3584+(l&1)*128, st3=((w&3)*64+8*(p&3))*3584+fb*256; OPQV(vt3); OPQS(st3);
  u32 vo=FX((u32)((g&1)*16384+(m+16*(2*(head&1)+(g>>1)))*16)); OPQV(vo);
  u32 soO=WS_O+((r0/16+2*mh)*16+(cb^(4*SWO))*2+(head>>1))*1024; OPQS(soO);
  u32 soH=WS_H+((r02/16+mt2)*16+(cb2^(4*SWH))*2+kh)*1024; OPQS(soH);
  u32 soS=STL? WS_ST+(p*8+cb2)*2048+kh*1024+rb2*512 : WS_ST+((p*8+cb2)*128+rb2*64)*16+kh*8; OPQS(soS);
  u32 m16=STL? (u32)(m*8+mt2*128) : FX((u32)(m*16+mt2*256)); OPQV(m16);
  u32 vhh=act3? FX((u32)(nh*57344+(m+16*(2*(w&1)+hi))*16)) : 0x70000000u; OPQV(vhh);
  u32 shh=WS_HH+((p*8)*56+(SWHH?(fb>=7?fb-7:fb+7):fb)*4+(w>>1))*1024; OPQS(shh);
  u32x4 arm={~0u,~0u,~0u,~0u}; OPQV(arm);
  const f32x4 z4={0,0,0,0};
  f32x4 aq[3], ak[3], av[3], sa[3];
  bf16x8 xa[3], ya[3], wa[3], xb[3], wb[3];
  SB;
  DMA(rsX,voffX,sX[0],128,wK,32768);
  DMA(rsW,voffW,sW[0],197632,wK,49152);
  DMA(rsW,voffW,sW[0],198656,wK,57344);
  DMA(rsW,voffW,sW[0],196608,wK,40960);
  DMA(rsX,voffH,sHB,0,wK,139264);
  SB;
  SB;
  asm volatile("s_waitcnt vmcnt(5)" :  :: "memory");
  BAR();
  asm volatile("s_waitcnt lgkmcnt(0)" : "+s"(tok)); tok+=0x9E3779B97F4A7C15ull; OPQS(tok);
  asm volatile("; MARK st0 landed");
  STAMP(1);
  wa[1]=trfrag(wA+8192,wA+8704);
  xa[0]=l128(a0A); a0A+=st0;
  wa[2]=trfrag(wA+16384,wA+16896);
  xa[1]=l128(vxA+0);
  wa[0]=trfrag(wA+0,wA+512);
  xa[2]=l128(vxA+2048);
  SB;
  ak[0]=mfma32(wa[1],xa[0],z4);
  wb[1]=trfrag(wB+8192,wB+8704);
  xb[0]=l128(a0B); a0B+=st0;
  SB;
  av[0]=mfma32(xa[0],wa[2],z4);
  wb[2]=trfrag(wB+16384,wB+16896);
  xb[1]=l128(vxB+0);
  SB;
  ak[1]=mfma32(wa[1],xa[1],z4);
  wb[0]=trfrag(wB+0,wB+512);
  xb[2]=l128(vxB+2048);
  SB;
  av[1]=mfma32(xa[1],wa[2],z4);
  ya[0]=asb((asq(xa[0])&yM)|yO);
  SB;
  __builtin_amdgcn_s_setprio(1);
  aq[1]=mfma32(wa[0],xa[1],z4);
  DMA(rsX,voffX,sX[1],0,wK,65536);
  SB;
  aq[0]=mfma32(wa[0],xa[0],z4);
  ya[1]=asb((asq(xa[1])&yM)|yO);
  DMA(rsW,voffW,sW[1],1024,wK,81920);
  SB;
  sa[0]=mfma32(ya[0],xa[0],z4);
  DMA(rsW,voffW,sW[1],2048,wK,90112);
  SB;
  ak[2]=mfma32(wa[1],xa[2],z4);
  ya[2]=asb((asq(xa[2])&yM)|yO);
  DMA(rsW,voffW,sW[1],0,wK,73728);
  SB;
  sa[1]=mfma32(ya[1],xa[1],z4);
  DMA(rsX,voffX,sX[1],128,wK,98304);
  SB;
  __builtin_amdgcn_s_setprio(0);
  av[2]=mfma32(xa[2],wa[2],z4);
  DMA(rsW,voffW,sW[1],197632,wK,114688);
  SB;
  sa[2]=mfma32(ya[2],xa[2],z4);
  DMA(rsW,voffW,sW[1],198656,wK,122880);
  SB;
  aq[2]=mfma32(wa[0],xa[2],z4);
  DMA(rsW,voffW,sW[1],196608,wK,106496);
  SB;
  asm volatile("s_waitcnt vmcnt(9)" :  :: "memory");
  asm volatile("" :: "v"(cpf));
  BAR();
  asm volatile("; MARK p1 mid0");
  __builtin_amdgcn_s_setprio(3);
  ak[0]=mfma32(wb[1],xb[0],ak[0]);
  wa[1]=trfrag(wA+40960,wA+41472);
  xa[0]=l128(a0A); a0A+=st0;
  SB;
  av[0]=mfma32(xb[0],wb[2],av[0]);
  wa[2]=trfrag(wA+49152,wA+49664);
  xa[1]=l128(vxA+32768);
  SB;
  ak[1]=mfma32(wb[1],xb[1],ak[1]);
  wa[0]=trfrag(wA+32768,wA+33280);
  xa[2]=l128(vxA+34816);
  SB;
  av[1]=mfma32(xb[1],wb[2],av[1]);
  DMA(rsX,voffX,sX[2],0,wK,0);
  SB;
  aq[1]=mfma32(wb[0],xb[1],aq[1]);
  DMA(rsW,voffW,sW[2],1024,wK,16384);
  SB;
  aq[0]=mfma32(wb[0],xb[0],aq[0]);
  DMA(rsW,voffW,sW[2],2048,wK,24576);
  SB;
  ak[2]=mfma32(wb[1],xb[2],ak[2]);
  DMA(rsW,voffW,sW[2],0,wK,8192);
  SB;
  __builtin_amdgcn_s_setprio(2);
  av[2]=mfma32(xb[2],wb[2],av[2]);
  SB;
  aq[2]=mfma32(wb[0],xb[2],aq[2]);
  SB;
  ak[0]=mfma32(wa[1],xa[0],ak[0]);
  wb[1]=trfrag(wB+40960,wB+41472);
  xb[0]=l128(a0B); a0B+=st0;
  SB;
  av[0]=mfma32(xa[0],wa[2],av[0]);
  wb[2]=trfrag(wB+49152,wB+49664);
  xb[1]=l128(vxB+32768);
  SB;
  ak[1]=mfma32(wa[1],xa[1],ak[1]);
  wb[0]=trfrag(wB+32768,wB+33280);
  xb[2]=l128(vxB+34816);
  SB;
  av[1]=mfma32(xa[1],wa[2],av[1]);
  ya[0]=asb((asq(xa[0])&yM)|yO);
  SB;
  __builtin_amdgcn_s_setprio(1);
  aq[1]=mfma32(wa[0],xa[1],aq[1]);
  ST16(arm,rsS,vo,soO);
  SB;
  aq[0]=mfma32(wa[0],xa[0],aq[0]);
  ya[1]=asb((asq(xa[1])&yM)|yO);
  SB;
  sa[0]=mfma32(ya[0],xa[0],sa[0]);
  { u32x2 a2_={arm[0],arm[1]}; ST8(a2_,rsS,m16,soS); }
  SB;
  ak[2]=mfma32(wa[1],xa[2],ak[2]);
  ya[2]=asb((asq(xa[2])&yM)|yO);
  SB;
  sa[1]=mfma32(ya[1],xa[1],sa[1]);
  SB;
  __builtin_amdgcn_s_setprio(0);
  av[2]=mfma32(xa[2],wa[2],av[2]);
  SB;
  sa[2]=mfma32(ya[2],xa[2],sa[2]);
  SB;
  aq[2]=mfma32(wa[0],xa[2],aq[2]);
  SB;
  asm volatile("s_waitcnt vmcnt(10)" :  :: "memory");
  BAR();
  asm volatile("; MARK p1 mid1");
  __builtin_amdgcn_s_setprio(3);
  ak[0]=mfma32(wb[1],xb[0],ak[0]);
  u32 wA_w1_1=wA+65536; OPQV(wA_w1_1);
  wa[1]=trfrag(wA_w1_1+8192,wA_w1_1+8704);
  xa[0]=l128(a0A); a0A+=st0;
  SB;
  av[0]=mfma32(xb[0],wb[2],av[0]);
  wa[2]=trfrag(wA_w1_1+16384,wA_w1_1+16896);
  u32 vxA_w1_2=vxA+65536; OPQV(vxA_w1_2);
  xa[1]=l128(vxA_w1_2+0);
  SB;
  ak[1]=mfma32(wb[1],xb[1],ak[1]);
  wa[0]=trfrag(wA_w1_1+0,wA_w1_1+512);
  xa[2]=l128(vxA_w1_2+2048);
  SB;
  av[1]=mfma32(xb[1],wb[2],av[1]);
  DMA(rsX,voffX,sX[2],128,wK,32768);
  SB;
  aq[1]=mfma32(wb[0],xb[1],aq[1]);
  DMA(rsW,voffW,sW[2],197632,wK,49152);
  SB;
  aq[0]=mfma32(wb[0],xb[0],aq[0]);
  DMA(rsW,voffW,sW[2],198656,wK,57344);
  SB;
  ak[2]=mfma32(wb[1],xb[2],ak[2]);
  DMA(rsW,voffW,sW[2],196608,wK,40960);
  SB;
  __builtin_amdgcn_s_setprio(2);
  av[2]=mfma32(xb[2],wb[2],av[2]);
  SB;
  aq[2]=mfma32(wb[0],xb[2],aq[2]);
  SB;
  ak[0]=mfma32(wa[1],xa[0],ak[0]);
  u32 wB_w1_3=wB+65536; OPQV(wB_w1_3);
  wb[1]=trfrag(wB_w1_3+8192,wB_w1_3+8704);
  xb[0]=l128(a0B); a0B+=st0;
  SB;
  av[0]=mfma32(xa[0],wa[2],av[0]);
  wb[2]=trfrag(wB_w1_3+16384,wB_w1_3+16896);
  u32 vxB_w1_4=vxB+65536; OPQV(vxB_w1_4);
  xb[1]=l128(vxB_w1_4+0);
  SB;
  ak[1]=mfma32(wa[1],xa[1],ak[1]);
  wb[0]=trfrag(wB_w1_3+0,wB_w1_3+512);
  xb[2]=l128(vxB_w1_4+2048);
  SB;
  av[1]=mfma32(xa[1],wa[2],av[1]);
  ya[0]=asb((asq(xa[0])&yM)|yO);
  SB;
  __builtin_amdgcn_s_setprio(1);
  aq[1]=mfma32(wa[0],xa[1],aq[1]);
  SB;
  aq[0]=mfma32(wa[0],xa[0],aq[0]);
  ya[1]=asb((asq(xa[1])&yM)|yO);
  SB;
  sa[0]=mfma32(ya[0],xa[0],sa[0]);
  SB;
  ak[2]=mfma32(wa[1],xa[2],ak[2]);
  ya[2]=asb((asq(xa[2])&yM)|yO);
  SB;
  sa[1]=mfma32(ya[1],xa[1],sa[1]);
  SB;
  __builtin_amdgcn_s_setprio(0);
  av[2]=mfma32(xa[2],wa[2],av[2]);
  SB;
  sa[2]=mfma32(ya[2],xa[2],sa[2]);
  SB;
  aq[2]=mfma32(wa[0],xa[2],aq[2]);
  SB;
  asm volatile("s_waitcnt vmcnt(10)" :  :: "memory");
  BAR();
  asm volatile("; MARK p1 mid2");
  __builtin_amdgcn_s_setprio(3);
  ak[0]=mfma32(wb[1],xb[0],ak[0]);
  wa[1]=trfrag(wA_w1_1+40960,wA_w1_1+41472);
  xa[0]=l128(a0A); a0A+=st4;
  SB;
  av[0]=mfma32(xb[0],wb[2],av[0]);
  wa[2]=trfrag(wA_w1_1+49152,wA_w1_1+49664);
  xa[1]=l128(vxA_w1_2+32768);
  SB;
  ak[1]=mfma32(wb[1],xb[1],ak[1]);
  wa[0]=trfrag(wA_w1_1+32768,wA_w1_1+33280);
  xa[2]=l128(vxA_w1_2+34816);
  SB;
  av[1]=mfma32(xb[1],wb[2],av[1]);
  DMA(rsX,voffX,sX[3],0,wK,65536);
  SB;
  aq[1]=mfma32(wb[0],xb[1],aq[1]);
  DMA(rsW,voffW,sW[3],1024,wK,81920);
  SB;
  aq[0]=mfma32(wb[0],xb[0],aq[0]);
  DMA(rsW,voffW,sW[3],2048,wK,90112);
  SB;
  ak[2]=mfma32(wb[1],xb[2],ak[2]);
  DMA(rsW,voffW,sW[3],0,wK,73728);
  SB;
  __builtin_amdgcn_s_setprio(2);
  av[2]=mfma32(xb[2],wb[2],av[2]);
  { const u32 o3=(384u-(u32)(u64)((w<4)?wg:wu))&1023u; const u32 t_=o3+(l>=7?l-7:l)*1024; const u32 c_=t_>=3584?t_-3584:t_;
    tc[9]=(l<14 && (MAP||(c_>=1792)==(xh!=0)))? o3+(((w&3)*32+tb)*14+l)*1024 : OOB; }
  TCHB(tc[9],rsG,tc[9],0);
  { const u32 o2=(384u-(u32)(u64)wo)&1023u; tc[10]=(l<2 && (MAP||R2||(o2>>9)==(u32)xc))? o2+(uw*2+l)*1024 : OOB; }
  TCHB(tc[10],rsO,tc[10],0);
  SB;
  aq[2]=mfma32(wb[0],xb[2],aq[2]);
  SB;
  ak[0]=mfma32(wa[1],xa[0],ak[0]);
  wb[1]=trfrag(wB_w1_3+40960,wB_w1_3+41472);
  xb[0]=l128(a0B); a0B+=st4;
  SB;
  av[0]=mfma32(xa[0],wa[2],av[0]);
  wb[2]=trfrag(wB_w1_3+49152,wB_w1_3+49664);
  xb[1]=l128(vxB_w1_4+32768);
  SB;
  ak[1]=mfma32(wa[1],xa[1],ak[1]);
  wb[0]=trfrag(wB_w1_3+32768,wB_w1_3+33280);
  xb[2]=l128(vxB_w1_4+34816);
  SB;
  av[1]=mfma32(xa[1],wa[2],av[1]);
  ya[0]=asb((asq(xa[0])&yM)|yO);
  SB;
  __builtin_amdgcn_s_setprio(1);
  aq[1]=mfma32(wa[0],xa[1],aq[1]);
  SB;
  aq[0]=mfma32(wa[0],xa[0],aq[0]);
  ya[1]=asb((asq(xa[1])&yM)|yO);
  SB;
  sa[0]=mfma32(ya[0],xa[0],sa[0]);
  SB;
  ak[2]=mfma32(wa[1],xa[2],ak[2]);
  ya[2]=asb((asq(xa[2])&yM)|yO);
  SB;
  sa[1]=mfma32(ya[1],xa[1],sa[1]);
  SB;
  __builtin_amdgcn_s_setprio(0);
  av[2]=mfma32(xa[2],wa[2],av[2]);
  SB;
  sa[2]=mfma32(ya[2],xa[2],sa[2]);
  SB;
  aq[2]=mfma32(wa[0],xa[2],aq[2]);
  SB;
  asm volatile("s_waitcnt vmcnt(12)" :  :: "memory");
  BAR();
  asm volatile("; MARK p1 mid3");
  __builtin_amdgcn_s_setprio(3);
  ak[0]=mfma32(wb[1],xb[0],ak[0]);
  wa[1]=trfrag(wA+8192,wA+8704);
  xa[0]=l128(a0A); a0A+=st0;
  SB;
  av[0]=mfma32(xb[0],wb[2],av[0]);
  wa[2]=trfrag(wA+16384,wA+16896);
  xa[1]=l128(vxA+0);
  SB;
  ak[1]=mfma32(wb[1],xb[1],ak[1]);
  wa[0]=trfrag(wA+0,wA+512);
  xa[2]=l128(vxA+2048);
  SB;
  av[1]=mfma32(xb[1],wb[2],av[1]);
  DMA(rsX,voffX,sX[3],128,wK,98304);
  SB;
  aq[1]=mfma32(wb[0],xb[1],aq[1]);
  DMA(rsW,voffW,sW[3],197632,wK,114688);
  SB;
  aq[0]=mfma32(wb[0],xb[0],aq[0]);
  DMA(rsW,voffW,sW[3],198656,wK,122880);
  SB;
  ak[2]=mfma32(wb[1],xb[2],ak[2]);
  DMA(rsW,voffW,sW[3],196608,wK,106496);
  SB;
  __builtin_amdgcn_s_setprio(2);
  av[2]=mfma32(xb[2],wb[2],av[2]);
  SB;
  aq[2]=mfma32(wb[0],xb[2],aq[2]);
  SB;
  ak[0]=mfma32(wa[1],xa[0],ak[0]);
  wb[1]=trfrag(wB+8192,wB+8704);
  xb[0]=l128(a0B); a0B+=st0;
  SB;
  av[0]=mfma32(xa[0],wa[2],av[0]);
  wb[2]=trfrag(wB+16384,wB+16896);
  xb[1]=l128(vxB+0);
  SB;
  ak[1]=mfma32(wa[1],xa[1],ak[1]);
  wb[0]=trfrag(wB+0,wB+512);
  xb[2]=l128(vxB+2048);
  SB;
  av[1]=mfma32(xa[1],wa[2],av[1]);
  ya[0]=asb((asq(xa[0])&yM)|yO);
  SB;
  __builtin_amdgcn_s_setprio(1);
  aq[1]=mfma32(wa[0],xa[1],aq[1]);
  SB;
  aq[0]=mfma32(wa[0],xa[0],aq[0]);
  ya[1]=asb((asq(xa[1])&yM)|yO);
  SB;
  sa[0]=mfma32(ya[0],xa[0],sa[0]);
  SB;
  ak[2]=mfma32(wa[1],xa[2],ak[2]);
  ya[2]=asb((asq(xa[2])&yM)|yO);
  SB;
  sa[1]=mfma32(ya[1],xa[1],sa[1]);
  SB;
  __builtin_amdgcn_s_setprio(0);
  av[2]=mfma32(xa[2],wa[2],av[2]);
  SB;
  sa[2]=mfma32(ya[2],xa[2],sa[2]);
  SB;
  aq[2]=mfma32(wa[0],xa[2],aq[2]);
  SB;
  asm volatile("s_waitcnt vmcnt(10)" :  :: "memory");
  BAR();
  FLAG(w,fl+128+(xh*8+idx)*8);
  asm volatile("; MARK p1 mid4");
  __builtin_amdgcn_s_setprio(3);
  ak[0]=mfma32(wb[1],xb[0],ak[0]);
  wa[1]=trfrag(wA+40960,wA+41472);
  xa[0]=l128(a0A); a0A+=st0;
  SB;
  av[0]=mfma32(xb[0],wb[2],av[0]);
  wa[2]=trfrag(wA+49152,wA+49664);
  xa[1]=l128(vxA+32768);
  SB;
  ak[1]=mfma32(wb[1],xb[1],ak[1]);
  wa[0]=trfrag(wA+32768,wA+33280);
  xa[2]=l128(vxA+34816);
  SB;
  av[1]=mfma32(xb[1],wb[2],av[1]);
  DMA(rsG3,voff3,s3[0],0,wK2,0);
  SB;
  aq[1]=mfma32(wb[0],xb[1],aq[1]);
  DMA(rsG3,voff3,s3[0],28672,wK2,1024);
  SB;
  aq[0]=mfma32(wb[0],xb[0],aq[0]);
  DMA(rsG3,voff3,s3[0],114688,wK2,16384);
  SB;
  ak[2]=mfma32(wb[1],xb[2],ak[2]);
  DMA(rsG3,voff3,s3[0],143360,wK2,17408);
  SB;
  __builtin_amdgcn_s_setprio(2);
  av[2]=mfma32(xb[2],wb[2],av[2]);
  SB;
  aq[2]=mfma32(wb[0],xb[2],aq[2]);
  SB;
  ak[0]=mfma32(wa[1],xa[0],ak[0]);
  wb[1]=trfrag(wB+40960,wB+41472);
  xb[0]=l128(a0B); a0B+=st0;
  SB;
  av[0]=mfma32(xa[0],wa[2],av[0]);
  wb[2]=trfrag(wB+49152,wB+49664);
  xb[1]=l128(vxB+32768);
  SB;
  ak[1]=mfma32(wa[1],xa[1],ak[1]);
  wb[0]=trfrag(wB+32768,wB+33280);
  xb[2]=l128(vxB+34816);
  SB;
  av[1]=mfma32(xa[1],wa[2],av[1]);
  ya[0]=asb((asq(xa[0])&yM)|yO);
  SB;
  __builtin_amdgcn_s_setprio(1);
  aq[1]=mfma32(wa[0],xa[1],aq[1]);
  SB;
  aq[0]=mfma32(wa[0],xa[0],aq[0]);
  ya[1]=asb((asq(xa[1])&yM)|yO);
  SB;
  sa[0]=mfma32(ya[0],xa[0],sa[0]);
  SB;
  ak[2]=mfma32(wa[1],xa[2],ak[2]);
  ya[2]=asb((asq(xa[2])&yM)|yO);
  SB;
  sa[1]=mfma32(ya[1],xa[1],sa[1]);
  SB;
  __builtin_amdgcn_s_setprio(0);
  av[2]=mfma32(xa[2],wa[2],av[2]);
  SB;
  sa[2]=mfma32(ya[2],xa[2],sa[2]);
  SB;
  aq[2]=mfma32(wa[0],xa[2],aq[2]);
  SB;
  asm volatile("s_waitcnt vmcnt(10)" :  :: "memory");
  BAR();
  asm volatile("; MARK p1 mid5");
  __builtin_amdgcn_s_setprio(3);
  ak[0]=mfma32(wb[1],xb[0],ak[0]);
  wa[1]=trfrag(wA_w1_1+8192,wA_w1_1+8704);
  xa[0]=l128(a0A); a0A+=st0;
  SB;
  av[0]=mfma32(xb[0],wb[2],av[0]);
  wa[2]=trfrag(wA_w1_1+16384,wA_w1_1+16896);
  xa[1]=l128(vxA_w1_2+0);
  SB;
  ak[1]=mfma32(wb[1],xb[1],ak[1]);
  wa[0]=trfrag(wA_w1_1+0,wA_w1_1+512);
  xa[2]=l128(vxA_w1_2+2048);
  SB;
  av[1]=mfma32(xb[1],wb[2],av[1]);
  DMA(rsG3,voff3,s3[0],229376,wK2,32768);
  SB;
  aq[1]=mfma32(wb[0],xb[1],aq[1]);
  DMA(rsG3,voff3,s3[0],258048,wK2,33792);
  SB;
  aq[0]=mfma32(wb[0],xb[0],aq[0]);
  DMA(rsG3,voff3,s3[0],344064,wK2,49152);
  SB;
  ak[2]=mfma32(wb[1],xb[2],ak[2]);
  DMA(rsG3,voff3,s3[0],372736,wK2,50176);
  SB;
  __builtin_amdgcn_s_setprio(2);
  av[2]=mfma32(xb[2],wb[2],av[2]);
  SB;
  aq[2]=mfma32(wb[0],xb[2],aq[2]);
  SB;
  ak[0]=mfma32(wa[1],xa[0],ak[0]);
  wb[1]=trfrag(wB_w1_3+8192,wB_w1_3+8704);
  xb[0]=l128(a0B); a0B+=st0;
  SB;
  av[0]=mfma32(xa[0],wa[2],av[0]);
  wb[2]=trfrag(wB_w1_3+16384,wB_w1_3+16896);
  xb[1]=l128(vxB_w1_4+0);
  SB;
  ak[1]=mfma32(wa[1],xa[1],ak[1]);
  wb[0]=trfrag(wB_w1_3+0,wB_w1_3+512);
  xb[2]=l128(vxB_w1_4+2048);
  SB;
  av[1]=mfma32(xa[1],wa[2],av[1]);
  ya[0]=asb((asq(xa[0])&yM)|yO);
  SB;
  __builtin_amdgcn_s_setprio(1);
  aq[1]=mfma32(wa[0],xa[1],aq[1]);
  SB;
  aq[0]=mfma32(wa[0],xa[0],aq[0]);
  ya[1]=asb((asq(xa[1])&yM)|yO);
  SB;
  sa[0]=mfma32(ya[0],xa[0],sa[0]);
  SB;
  ak[2]=mfma32(wa[1],xa[2],ak[2]);
  ya[2]=asb((asq(xa[2])&yM)|yO);
  SB;
  sa[1]=mfma32(ya[1],xa[1],sa[1]);
  SB;
  __builtin_amdgcn_s_setprio(0);
  av[2]=mfma32(xa[2],wa[2],av[2]);
  SB;
  sa[2]=mfma32(ya[2],xa[2],sa[2]);
  SB;
  aq[2]=mfma32(wa[0],xa[2],aq[2]);
  SB;
  asm volatile("s_waitcnt vmcnt(8)" :  :: "memory");
  bf16x8 W3[32]; f32x4 cs4;
  const f32x16 z16={0,0,0,0,0,0,0,0,0,0,0,0,0,0,0,0};
  u32x4 selA; { const u32 o_=((q4&1)==nh)?0x3f803f80u:0u; selA=u32x4{o_,o_,o_,o_}; } OPQV(selA);
  u32 a3=(nh*2+(w>>2))*4096+(4*hi+q4)*128+(((w&3)^((((q4>>1)&1)<<1)|nh))*32)+p4*8; OPQV(a3);
  asm volatile("s_waitcnt vmcnt(4)" :  :: "memory");
  asm volatile("" :: "v"(tc[9]));
  asm volatile("" :: "v"(tc[10]));
  BAR();
  asm volatile("; MARK p1 mid6");
  __builtin_amdgcn_s_setprio(3);
  ak[0]=mfma32(wb[1],xb[0],ak[0]);
  wa[1]=trfrag(wA_w1_1+40960,wA_w1_1+41472);
  xa[0]=l128(a0A);
  SB;
  av[0]=mfma32(xb[0],wb[2],av[0]);
  wa[2]=trfrag(wA_w1_1+49152,wA_w1_1+49664);
  xa[1]=l128(vxA_w1_2+32768);
  SB;
  ak[1]=mfma32(wb[1],xb[1],ak[1]);
  wa[0]=trfrag(wA_w1_1+32768,wA_w1_1+33280);
  xa[2]=l128(vxA_w1_2+34816);
  SB;
  av[1]=mfma32(xb[1],wb[2],av[1]);
  DMA(rsO,voffO,sO[0],0,wK,65536);
  SB;
  aq[1]=mfma32(wb[0],xb[1],aq[1]);
  DMA(rsO,voffO,sO[1],0,wK,73728);
  W3[0]=trfrag(a3+0,a3+2048);
  SB;
  aq[0]=mfma32(wb[0],xb[0],aq[0]);
  DMA(rsO,voffO,sO[2],0,wK,81920);
  W3[1]=trfrag(a3+1024,a3+3072);
  SB;
  ak[2]=mfma32(wb[1],xb[2],ak[2]);
  DMA(rsO,voffO,sO[3],0,wK,90112);
  W3[2]=trfrag(a3+16384,a3+18432);
  SB;
  __builtin_amdgcn_s_setprio(2);
  av[2]=mfma32(xb[2],wb[2],av[2]);
  W3[3]=trfrag(a3+17408,a3+19456);
  SB;
  aq[2]=mfma32(wb[0],xb[2],aq[2]);
  SB;
  ak[0]=mfma32(wa[1],xa[0],ak[0]);
  wb[1]=trfrag(wB_w1_3+40960,wB_w1_3+41472);
  xb[0]=l128(a0B);
  SB;
  av[0]=mfma32(xa[0],wa[2],av[0]);
  wb[2]=trfrag(wB_w1_3+49152,wB_w1_3+49664);
  xb[1]=l128(vxB_w1_4+32768);
  SB;
  ak[1]=mfma32(wa[1],xa[1],ak[1]);
  wb[0]=trfrag(wB_w1_3+32768,wB_w1_3+33280);
  xb[2]=l128(vxB_w1_4+34816);
  SB;
  av[1]=mfma32(xa[1],wa[2],av[1]);
  ya[0]=asb((asq(xa[0])&yM)|yO);
  SB;
  __builtin_amdgcn_s_setprio(1);
  aq[1]=mfma32(wa[0],xa[1],aq[1]);
  SB;
  aq[0]=mfma32(wa[0],xa[0],aq[0]);
  ya[1]=asb((asq(xa[1])&yM)|yO);
  SB;
  sa[0]=mfma32(ya[0],xa[0],sa[0]);
  SB;
  ak[2]=mfma32(wa[1],xa[2],ak[2]);
  ya[2]=asb((asq(xa[2])&yM)|yO);
  SB;
  sa[1]=mfma32(ya[1],xa[1],sa[1]);
  SB;
  __builtin_amdgcn_s_setprio(0);
  av[2]=mfma32(xa[2],wa[2],av[2]);
  SB;
  sa[2]=mfma32(ya[2],xa[2],sa[2]);
  SB;
  aq[2]=mfma32(wa[0],xa[2],aq[2]);
  SB;
  { const u32 pa=(g==q4)? 155904+(mh*48+m)*16+head*4 : 157440+l*4; const u32 ps=(g==q4)?256u:0u;
    _Pragma("unroll") for(int t=0;t<3;t++){ const float a_=(m&1)?sa[t][1]:sa[t][0], b_=(m&1)?sa[t][3]:sa[t][2]; *(LAS float*)(pa+t*ps)=(m&2)?b_:a_; } }
  asm volatile("s_waitcnt vmcnt(4)" :  :: "memory");
  BAR();
  asm volatile("; MARK p1 mid7");
  ak[0]=mfma32(wb[1],xb[0],ak[0]);
  const u32 tl=min(g,2);
  const f32x4 pp=lf4(155904+((mh*3+tl)*16+m)*16);
  const u32 wst=157696+w*512;
  u64 ff;
  DMA(rsG3,voff3,s3[1],0,wK2,0);
  SB;
  av[0]=mfma32(xb[0],wb[2],av[0]);
  DMA(rsG3,voff3,s3[1],28672,wK2,1024);
  W3[4]=trfrag(a3+32768,a3+34816);
  SB;
  aq[0]=mfma32(wb[0],xb[0],aq[0]);
  { const float mean=(pp[1]+pp[3])*(1.f/D); const float var=fmaxf((pp[0]+pp[2])*(1.f/D)-mean*mean,0.f);
    *(LAS float*)(wst+(tl*16+m)*4)=-mean; *(LAS float*)(wst+256+(tl*16+m)*4)=__builtin_amdgcn_rsqf(var+EPS); }
  DMA(rsG3,voff3,s3[1],114688,wK2,16384);
  W3[5]=trfrag(a3+33792,a3+35840);
  SB;
  ak[1]=mfma32(wb[1],xb[1],ak[1]);
  const int par=l&1;
  const bool c0=(4*g+par)<=m, c1=(4*g+par+2)<=m;
  float nm[3], rq[2], rk0[2], rk1[2]; f32x4 n4[3];
  const u32 k0a=wst+256+(4*g+par+(c0?16:0))*4, k1a=wst+256+(4*g+par+2+(c1?16:0))*4;
  _Pragma("unroll") for(int t=0;t<3;t++){ nm[t]=lf1(wst+(t*16+m)*4); n4[t]=lf4(wst+(t*16+4*g)*4); }
  DMA(rsG3,voff3,s3[1],143360,wK2,17408);
  SB;
  aq[1]=mfma32(wb[0],xb[1],aq[1]);
  _Pragma("unroll") for(int t=0;t<2;t++){ rq[t]=lf1(wst+256+(t*16+16+m)*4); rk0[t]=lf1(k0a+t*64); rk1[t]=lf1(k1a+t*64); }
  const bool noprev=(r0==0)&&(mh==0);
  float bi0=(noprev&&!c0)?-INFINITY:0.f, bi1=(noprev&&!c1)?-INFINITY:0.f; OPQV(bi0); OPQV(bi1);
  u32 ps[4]; { const u32 e0=par?0x01000c0cu:0x0c0c0100u, e1=par?0x03020c0cu:0x0c0c0302u, zz=0x0c0c0c0cu;
    ps[0]=c0?zz:e0; ps[1]=c1?zz:e1; ps[2]=c0?e0:zz; ps[3]=c1?e1:zz; _Pragma("unroll") for(int i=0;i<4;i++) OPQV(ps[i]); }
  DMA(rsO,voffO,sO[0],262144,wK,98304);
  SB;
  av[1]=mfma32(xb[1],wb[2],av[1]);
  float csq[4], csk[4];
  _Pragma("unroll") for(int i=0;i<4;i++){ csk[i]=bc0(ak[0][i]); }
  const float csv=row0(av[0][0]);
  DMA(rsO,voffO,sO[1],262144,wK,106496);
  W3[6]=trfrag(a3+49152,a3+51200);
  SB;
  ak[2]=mfma32(wb[1],xb[2],ak[2]);
  _Pragma("unroll") for(int i=0;i<4;i++){ csq[i]=bc0(aq[0][i]); }
  DMA(rsO,voffO,sO[2],262144,wK,114688);
  W3[7]=trfrag(a3+50176,a3+52224);
  SB;
  aq[2]=mfma32(wb[0],xb[2],aq[2]);
  float rc0[2], rc1[2];
  _Pragma("unroll") for(int t=0;t<2;t++){ const float c_=rq[t]*(0.25f*1.44269504f); rc0[t]=rk0[t]*c_; rc1[t]=rk1[t]*c_; }
  DMA(rsO,voffO,sO[3],262144,wK,122880);
  SB;
  av[2]=mfma32(xb[2],wb[2],av[2]);
  LD8(ff,rsS,m*8,fl+128,0);
  SB;
  asm volatile("; MARK p1 loop end");
  STAMP(2);
  bf16x4 qb[3], kb[3], vb[3];
  _Pragma("unroll") for(int t=0;t<3;t++){
    kb[t]=pk4(fmaf(nm[t],csk[0],ak[t][0]), fmaf(nm[t],csk[1],ak[t][1]), fmaf(nm[t],csk[2],ak[t][2]), fmaf(nm[t],csk[3],ak[t][3]));
    if(t>0) qb[t]=pk4(fmaf(nm[t],csq[0],aq[t][0]), fmaf(nm[t],csq[1],aq[t][1]), fmaf(nm[t],csq[2],aq[t][2]), fmaf(nm[t],csq[3],aq[t][3]));
  }
  f32x4 sc[2], sp[2];
  _Pragma("unroll") for(int t=1;t<3;t++){ sc[t-1]=mfma16(kb[t],qb[t],z4); sp[t-1]=mfma16(kb[t-1],qb[t],z4); }
  _Pragma("unroll") for(int t=0;t<3;t++)
    vb[t]=pk4(fmaf(n4[t][0],csv,av[t][0]), fmaf(n4[t][1],csv,av[t][1]), fmaf(n4[t][2],csv,av[t][2]), fmaf(n4[t][3],csv,av[t][3]));
  _Pragma("unroll") for(int t=0;t<3;t++) OPQV(vb[t]);
  SB;
  BAR();
  DMA(rsG3,voff3,s3[1],229376,wK2,32768);
  DMA(rsG3,voff3,s3[1],258048,wK2,33792);
  DMA(rsG3,voff3,s3[1],344064,wK2,49152);
  DMA(rsG3,voff3,s3[1],372736,wK2,50176);
  ST16(arm,rsS,g16,soH);
  SB;
  u32x4 ov;
  { float s0[2], s1[2], p0[2], p1[2], mx[2], sm[2];
    _Pragma("unroll") for(int t=0;t<2;t++){
      const float a0=sc[t][0],a1=sc[t][1],a2=sc[t][2],a3=sc[t][3],b0=sp[t][0],b1=sp[t][1],b2=sp[t][2],b3=sp[t][3];
      const float u0=sel(par,a1,a0), v0=sel(par,b1,b0), u1=sel(par,a3,a2), v1=sel(par,b3,b2);
      s0[t]=sel(c0,u0,v0); s1[t]=sel(c1,u1,v1);
      if(t==0){ s0[t]=fmaf(s0[t],rc0[t],bi0); s1[t]=fmaf(s1[t],rc1[t],bi1); } else { s0[t]*=rc0[t]; s1[t]*=rc1[t]; }
      mx[t]=fmx(s0[t],s1[t]);
    }
    { auto r=__builtin_amdgcn_permlane16_swap(asu(mx[0]),asu(mx[1]),false,false); u32 y=asu(fmx(asf(r[0]),asf(r[1])));
      auto r2=__builtin_amdgcn_permlane32_swap(y,y,false,false); y=asu(fmx(asf(r2[0]),asf(r2[1])));
      auto r3=__builtin_amdgcn_permlane16_swap(y,y,false,false); mx[0]=asf(r3[0]); mx[1]=asf(r3[1]); }
    _Pragma("unroll") for(int t=0;t<2;t++){ p0[t]=__builtin_amdgcn_exp2f(s0[t]-mx[t]); p1[t]=__builtin_amdgcn_exp2f(s1[t]-mx[t]); sm[t]=p0[t]+p1[t]; p0[t]*=rk0[t]; p1[t]*=rk1[t]; }
    { auto r=__builtin_amdgcn_permlane16_swap(asu(sm[0]),asu(sm[1]),false,false); u32 y=asu(asf(r[0])+asf(r[1]));
      auto r2=__builtin_amdgcn_permlane32_swap(y,y,false,false); y=asu(__builtin_amdgcn_rcpf(asf(r2[0])+asf(r2[1])));
      auto r3=__builtin_amdgcn_permlane16_swap(y,y,false,false); sm[0]=asf(r3[0]); sm[1]=asf(r3[1]); }
    u32 op[2][2];
    _Pragma("unroll") for(int t=0;t<2;t++){
      const u32 u=pk2(p0[t]*sm[t],p1[t]*sm[t]);
      u32x4 pb; _Pragma("unroll") for(int i=0;i<4;i++) pb[i]=__builtin_amdgcn_perm(u,u,ps[i]);
      f32x4 o=mfma32(cat(vb[t],vb[t+1]),asb(pb),z4);
      op[t][0]=pk2(o[0],o[1]); op[t][1]=pk2(o[2],o[3]);
    }
    auto e0=__builtin_amdgcn_permlane16_swap(op[0][0],op[1][0],false,false);
    auto e1=__builtin_amdgcn_permlane16_swap(op[0][1],op[1][1],false,false);
    ov=u32x4{(u32)e0[0],(u32)e1[0],(u32)e0[1],(u32)e1[1]};
  }
  ST16(ov,rsS,vo,soO);
  SB;
  asm volatile("; MARK o stored");
  STAMP(3);
  u32 a2[4];
  _Pragma("unroll") for(int a=0;a<4;a++){ a2[a]=65536+kh*32768+(8*g+q4)*128+((((a+2*kh)&3)^sw1)*32)+p4*8; OPQV(a2[a]); }
  bf16x8 A2[8][4]; u32x4 ob[8]; f32x4 acc2[4]; u32x2 xr0, xr1;
  u32 so2[4]; _Pragma("unroll") for(int b=0;b<4;b++){ so2[b]=WS_O+((r02/16+mt2)*16+8*(kh^SWO)+2*Bk[b])*1024; OPQS(so2[b]); }
    const u32 vxr=m*1024+g*8, sxr=(r02+16*mt2)*1024+cb2*128+kh*64;
  LD8P(xr0,rsX,vxr,sxr,0);
  LD8P(xr1,rsX,vxr,sxr,32);
  CHKF(8,ff,m*8,fl+128);
  FLAG(w+idx+xh,fl);
  SB;
  cs4=mfma32(asb(selA),W3[0],z4); OPQV(cs4);
  cs4=mfma32(asb(selA),W3[1],cs4); OPQV(cs4);
  cs4=mfma32(asb(selA),W3[2],cs4); OPQV(cs4);
  cs4=mfma32(asb(selA),W3[3],cs4); OPQV(cs4);
  cs4=mfma32(asb(selA),W3[4],cs4); OPQV(cs4);
  cs4=mfma32(asb(selA),W3[5],cs4); OPQV(cs4);
  cs4=mfma32(asb(selA),W3[6],cs4); OPQV(cs4);
  cs4=mfma32(asb(selA),W3[7],cs4); OPQV(cs4);
  SB;
  BAR();
  W3[8]=trfrag(a3+0,a3+2048);
  W3[9]=trfrag(a3+1024,a3+3072);
  W3[10]=trfrag(a3+16384,a3+18432);
  W3[11]=trfrag(a3+17408,a3+19456);
  SB;
  A2[0][0]=trfrag(a2[0]+0,a2[0]+512);
  A2[0][1]=trfrag(a2[1]+0,a2[1]+512);
  A2[0][2]=trfrag(a2[2]+0,a2[2]+512);
  A2[0][3]=trfrag(a2[3]+0,a2[3]+512);
  A2[1][0]=trfrag(a2[0]+4096,a2[0]+4608);
  A2[1][1]=trfrag(a2[1]+4096,a2[1]+4608);
  A2[1][2]=trfrag(a2[2]+4096,a2[2]+4608);
  A2[1][3]=trfrag(a2[3]+4096,a2[3]+4608);
  A2[2][0]=trfrag(a2[0]+8192,a2[0]+8704);
  A2[2][1]=trfrag(a2[1]+8192,a2[1]+8704);
  A2[2][2]=trfrag(a2[2]+8192,a2[2]+8704);
  A2[2][3]=trfrag(a2[3]+8192,a2[3]+8704);
  A2[3][0]=trfrag(a2[0]+12288,a2[0]+12800);
  A2[3][1]=trfrag(a2[1]+12288,a2[1]+12800);
  A2[3][2]=trfrag(a2[2]+12288,a2[2]+12800);
  A2[3][3]=trfrag(a2[3]+12288,a2[3]+12800);
  cs4=mfma32(asb(selA),W3[8],cs4); OPQV(cs4);
  cs4=mfma32(asb(selA),W3[9],cs4); OPQV(cs4);
  cs4=mfma32(asb(selA),W3[10],cs4); OPQV(cs4);
  cs4=mfma32(asb(selA),W3[11],cs4); OPQV(cs4);
  SB;
  asm volatile("s_waitcnt vmcnt(4)" :  :: "memory");
  BAR();
  asm volatile("; MARK o loads");
  STAMP(4);
  LD16(ob[0],rsS,g16,so2[0],0);
  LD16(ob[1],rsS,g16,so2[0],1024);
  LD16(ob[2],rsS,g16,so2[1],0);
  LD16(ob[3],rsS,g16,so2[1],1024);
  LD16(ob[4],rsS,g16,so2[2],0);
  LD16(ob[5],rsS,g16,so2[2],1024);
  LD16(ob[6],rsS,g16,so2[3],0);
  LD16(ob[7],rsS,g16,so2[3],1024);
  SB;
  W3[12]=trfrag(a3+32768,a3+34816);
  W3[13]=trfrag(a3+33792,a3+35840);
  W3[14]=trfrag(a3+49152,a3+51200);
  W3[15]=trfrag(a3+50176,a3+52224);
  DMA(rsG3,voff3,s3[2],0,wK2,0);
  DMA(rsG3,voff3,s3[2],28672,wK2,1024);
  DMA(rsG3,voff3,s3[2],114688,wK2,16384);
  DMA(rsG3,voff3,s3[2],143360,wK2,17408);
  cs4=mfma32(asb(selA),W3[12],cs4); OPQV(cs4);
  cs4=mfma32(asb(selA),W3[13],cs4); OPQV(cs4);
  cs4=mfma32(asb(selA),W3[14],cs4); OPQV(cs4);
  cs4=mfma32(asb(selA),W3[15],cs4); OPQV(cs4);
  SB;
  asm volatile("s_waitcnt vmcnt(15)" :  :: "memory");
  BAR();
  FLAG(w,fl+2560+(xh*8+idx)*8);
  DMA(rsG3,voff3,s3[2],229376,wK2,32768);
  DMA(rsG3,voff3,s3[2],258048,wK2,33792);
  DMA(rsG3,voff3,s3[2],344064,wK2,49152);
  DMA(rsG3,voff3,s3[2],372736,wK2,50176);
  { const u32 o3=(384u-(u32)(u64)wd)&1023u; tc[11]=(l<7 && (MAP||R4||(o3>>9)==(u32)xc))? o3+(uw*7+l)*1024 : OOB; }
  TCHB(tc[11],rsD,tc[11],0);
  asm volatile("; MARK o chk");
  STAMP(5);
  CHKR4(15,ob[0],ob[1],ob[2],ob[3],ob[4],ob[5],ob[6],ob[7],so2[0],so2[1],so2[2],so2[3]);
  asm volatile("; MARK o seen");
  STAMP(6);
  __builtin_amdgcn_s_setprio(3);
  acc2[0]=mfma32(A2[0][0],asb(ob[0]),z4);
  A2[4][0]=trfrag(a2[0]+16384,a2[0]+16896);
  SB;
  acc2[1]=mfma32(A2[0][1],asb(ob[0]),z4);
  A2[4][1]=trfrag(a2[1]+16384,a2[1]+16896);
  SB;
  acc2[2]=mfma32(A2[0][2],asb(ob[0]),z4);
  A2[4][2]=trfrag(a2[2]+16384,a2[2]+16896);
  SB;
  acc2[3]=mfma32(A2[0][3],asb(ob[0]),z4);
  A2[4][3]=trfrag(a2[3]+16384,a2[3]+16896);
  SB;
  acc2[0]=mfma32(A2[1][0],asb(ob[1]),acc2[0]);
  A2[5][0]=trfrag(a2[0]+20480,a2[0]+20992);
  SB;
  acc2[1]=mfma32(A2[1][1],asb(ob[1]),acc2[1]);
  A2[5][1]=trfrag(a2[1]+20480,a2[1]+20992);
  SB;
  acc2[2]=mfma32(A2[1][2],asb(ob[1]),acc2[2]);
  A2[5][2]=trfrag(a2[2]+20480,a2[2]+20992);
  SB;
  acc2[3]=mfma32(A2[1][3],asb(ob[1]),acc2[3]);
  A2[5][3]=trfrag(a2[3]+20480,a2[3]+20992);
  SB;
  CHKR3(13,ob[2],ob[3],ob[4],ob[5],ob[6],ob[7],so2[1],so2[2],so2[3]);
  __builtin_amdgcn_s_setprio(2);
  acc2[0]=mfma32(A2[2][0],asb(ob[2]),acc2[0]);
  A2[6][0]=trfrag(a2[0]+24576,a2[0]+25088);
  SB;
  acc2[1]=mfma32(A2[2][1],asb(ob[2]),acc2[1]);
  A2[6][1]=trfrag(a2[1]+24576,a2[1]+25088);
  SB;
  acc2[2]=mfma32(A2[2][2],asb(ob[2]),acc2[2]);
  A2[6][2]=trfrag(a2[2]+24576,a2[2]+25088);
  SB;
  acc2[3]=mfma32(A2[2][3],asb(ob[2]),acc2[3]);
  A2[6][3]=trfrag(a2[3]+24576,a2[3]+25088);
  SB;
  acc2[0]=mfma32(A2[3][0],asb(ob[3]),acc2[0]);
  A2[7][0]=trfrag(a2[0]+28672,a2[0]+29184);
  SB;
  acc2[1]=mfma32(A2[3][1],asb(ob[3]),acc2[1]);
  A2[7][1]=trfrag(a2[1]+28672,a2[1]+29184);
  SB;
  acc2[2]=mfma32(A2[3][2],asb(ob[3]),acc2[2]);
  A2[7][2]=trfrag(a2[2]+28672,a2[2]+29184);
  SB;
  acc2[3]=mfma32(A2[3][3],asb(ob[3]),acc2[3]);
  A2[7][3]=trfrag(a2[3]+28672,a2[3]+29184);
  SB;
  CHKR2(11,ob[4],ob[5],ob[6],ob[7],so2[2],so2[3]);
  __builtin_amdgcn_s_setprio(1);
  acc2[0]=mfma32(A2[4][0],asb(ob[4]),acc2[0]);
  SB;
  acc2[1]=mfma32(A2[4][1],asb(ob[4]),acc2[1]);
  SB;
  acc2[2]=mfma32(A2[4][2],asb(ob[4]),acc2[2]);
  SB;
  acc2[3]=mfma32(A2[4][3],asb(ob[4]),acc2[3]);
  SB;
  acc2[0]=mfma32(A2[5][0],asb(ob[5]),acc2[0]);
  SB;
  acc2[1]=mfma32(A2[5][1],asb(ob[5]),acc2[1]);
  SB;
  acc2[2]=mfma32(A2[5][2],asb(ob[5]),acc2[2]);
  SB;
  acc2[3]=mfma32(A2[5][3],asb(ob[5]),acc2[3]);
  SB;
  CHK2(9,ob[6],ob[7],g16,so2[3],0,so2[3],1024);
  __builtin_amdgcn_s_setprio(0);
  acc2[0]=mfma32(A2[6][0],asb(ob[6]),acc2[0]);
  SB;
  acc2[1]=mfma32(A2[6][1],asb(ob[6]),acc2[1]);
  SB;
  acc2[2]=mfma32(A2[6][2],asb(ob[6]),acc2[2]);
  SB;
  acc2[3]=mfma32(A2[6][3],asb(ob[6]),acc2[3]);
  SB;
  acc2[2]=mfma32(A2[7][2],asb(ob[7]),acc2[2]);
  SB;
  acc2[3]=mfma32(A2[7][3],asb(ob[7]),acc2[3]);
  SB;
  acc2[0]=mfma32(A2[7][0],asb(ob[7]),acc2[0]);
  SB;
  acc2[1]=mfma32(A2[7][1],asb(ob[7]),acc2[1]);
  SB;
  asm volatile("; MARK p2 mfma done");
  STAMP(7);
  { const u32 ra=131072+2*wK+l16; *(LAS f32x4*)(ra)=acc2[2]; *(LAS f32x4*)(ra+1024)=acc2[3]; }
  asm volatile("" : "+v"(xr0), "+v"(xr1) :: "memory");
  asm volatile("s_waitcnt vmcnt(5)" :  :: "memory");
  BAR();
  u32x4 hv; u32x2 sv;
  { const u32 ra=131072+(w^4)*2048+l16;
    f32x4 r0v=acc2[0]+f32x4{lo2f(xr0[0]),hi2f(xr0[0]),lo2f(xr0[1]),hi2f(xr0[1])}, r1v=acc2[1]+f32x4{lo2f(xr1[0]),hi2f(xr1[0]),lo2f(xr1[1]),hi2f(xr1[1])};
    r0v+=lf4(ra); r1v+=lf4(ra+1024);
    hv[0]=pk2(r0v[0],r0v[1]); hv[1]=pk2(r0v[2],r0v[3]); hv[2]=pk2(r1v[0],r1v[1]); hv[3]=pk2(r1v[2],r1v[3]);
  ST16(hv,rsS,g16,soH);
  f32x4 s4_=r0v+r1v, q4_=r0v*r0v+r1v*r1v;
    float s_=psum((s4_[0]+s4_[1])+(s4_[2]+s4_[3])), q_=psum((q4_[0]+q4_[1])+(q4_[2]+q4_[3])); sv=u32x2{asu(s_),asu(q_)}; }
  ST8(sv,rsS,m16,soS);
  SB;
  asm volatile("; MARK h stored");
  STAMP(8);
  LD8(ff,rsS,m*8,fl+2560,0);
  u32x4 hs[16], stv[2];
  u32 hw=((2*wh-4*rot)&15)*1024+l16; OPQV(hw);
  u32 b3=((l>>4)&1)*16384+((l&15)+16*hi)*16; OPQV(b3);
  bf16x8 bf[4]; f32x16 acc[2]; f32x4 cst; u32 hp[4];
  float ea[2], eu[2], et[2];
  u32 shl[8]; _Pragma("unroll") for(int i=0;i<8;i++){ shl[i]=WS_H+((p*8+i)*16+2*(wh^(4*SWH)))*1024; OPQS(shl[i]); }
  u32 sst = WS_ST+(p*8+w)*2048; OPQS(sst);
  u32 ast=155648+(l&31)*16; OPQV(ast);
  SB;
  DMA(rsG3,voff3,s3[3],0,wK2,65536);
  DMA(rsG3,voff3,s3[3],28672,wK2,66560);
  DMA(rsG3,voff3,s3[3],114688,wK2,81920);
  DMA(rsG3,voff3,s3[3],143360,wK2,82944);
  W3[16]=trfrag(a3+0,a3+2048);
  W3[17]=trfrag(a3+1024,a3+3072);
  W3[18]=trfrag(a3+16384,a3+18432);
  W3[19]=trfrag(a3+17408,a3+19456);
  SB;
  cs4=mfma32(asb(selA),W3[16],cs4); OPQV(cs4);
  cs4=mfma32(asb(selA),W3[17],cs4); OPQV(cs4);
  cs4=mfma32(asb(selA),W3[18],cs4); OPQV(cs4);
  cs4=mfma32(asb(selA),W3[19],cs4); OPQV(cs4);
  SB;
  asm volatile("s_waitcnt vmcnt(8)" :  :: "memory");
  BAR();
  W3[20]=trfrag(a3+32768,a3+34816);
  W3[21]=trfrag(a3+33792,a3+35840);
  W3[22]=trfrag(a3+49152,a3+51200);
  W3[23]=trfrag(a3+50176,a3+52224);
  asm volatile("; MARK h loads");
  STAMP(9);
  DMA(rsG3,voff3,s3[3],229376,wK2,98304);
  DMA(rsG3,voff3,s3[3],258048,wK2,99328);
  DMA(rsG3,voff3,s3[3],344064,wK2,114688);
  DMA(rsG3,voff3,s3[3],372736,wK2,115712);
  CHKF(8,ff,m*8,fl+2560);
  LD16(hs[0],rsS,g16,shl[0],0);
  LD16(hs[1],rsS,g16,shl[0],1024);
  LD16(hs[2],rsS,g16,shl[1],0);
  LD16(hs[3],rsS,g16,shl[1],1024);
  SB;
  cs4=mfma32(asb(selA),W3[20],cs4); OPQV(cs4);
  cs4=mfma32(asb(selA),W3[21],cs4); OPQV(cs4);
  cs4=mfma32(asb(selA),W3[22],cs4); OPQV(cs4);
  cs4=mfma32(asb(selA),W3[23],cs4); OPQV(cs4);
  SB;
  asm volatile("; MARK h chk");
  STAMP(10);
  CHKR2(2,hs[0],hs[1],hs[2],hs[3],shl[0],shl[1]);
  *(LAS u32x4*)(hw+0)=hs[0];
  *(LAS u32x4*)(hw+1024)=hs[1];
  CHK2(0,hs[2],hs[3],g16,shl[1],0,shl[1],1024);
  *(LAS u32x4*)(hw+16384)=hs[2];
  *(LAS u32x4*)(hw+17408)=hs[3];
  asm volatile("" :: "v"(tc[11]));
  BAR();
  asm volatile("; MARK h0 staged");
  STAMP(11);
  bf[0]=l128(b3+0);
  bf[1]=l128(b3+512);
  bf[2]=l128(b3+1024);
  SB;
  u32 a3_w1_5=a3+65536; OPQV(a3_w1_5);
  W3[24]=trfrag(a3_w1_5+0,a3_w1_5+2048);
  acc[0]=mfmaL(W3[0],bf[0],z16); OPQV(acc[0]);
  bf[3]=l128(b3+1536);
  SB;
  __builtin_amdgcn_s_setprio(3);
  const u32 voff4=l3*1024+((l7^(((((l3>>1)&1)<<1)|((l3>>2)&1))<<1))*16);
  const int rot7=((p&3)*2+rb)%7;
  u32 s4[7]; _Pragma("unroll") for(int b=0;b<7;b++){ const int k_=(b+rot7>=7)?b+rot7-7:b+rot7; s4[b]=(224*w4+32*k_)*1024+cb4*128; OPQS(s4[b]); }
  W3[25]=trfrag(a3_w1_5+1024,a3_w1_5+3072);
  LD16(hs[4],rsS,g16,shl[2],0);
  LD16(hs[5],rsS,g16,shl[2],1024);
  LD16(hs[6],rsS,g16,shl[3],0);
  LD16(hs[7],rsS,g16,shl[3],1024);
  acc[0]=mfmaL(W3[1],bf[1],acc[0]); OPQV(acc[0]);
  bf[0]=l128(b3+2048);
  SB;
  W3[26]=trfrag(a3_w1_5+16384,a3_w1_5+18432);
  LD16(stv[0],rsS,g16,sst,0);
  LD16(stv[1],rsS,g16,sst,1024);
  acc[0]=mfmaL(W3[2],bf[2],acc[0]); OPQV(acc[0]);
  bf[1]=l128(b3+2560);
  SB;
  W3[27]=trfrag(a3_w1_5+17408,a3_w1_5+19456);
  acc[0]=mfmaL(W3[3],bf[3],acc[0]); OPQV(acc[0]);
  bf[2]=l128(b3+3072);
  SB;
  LD16(hs[8],rsS,g16,shl[4],0);
  LD16(hs[9],rsS,g16,shl[4],1024);
  LD16(hs[10],rsS,g16,shl[5],0);
  LD16(hs[11],rsS,g16,shl[5],1024);
  acc[0]=mfmaL(W3[4],bf[0],acc[0]); OPQV(acc[0]);
  bf[3]=l128(b3+3584);
  SB;
  cs4=mfma32(asb(selA),W3[24],cs4); OPQV(cs4);
  SB;
  acc[0]=mfmaL(W3[5],bf[1],acc[0]); OPQV(acc[0]);
  bf[0]=l128(b3+4096);
  SB;
  LD16(hs[12],rsS,g16,shl[6],0);
  LD16(hs[13],rsS,g16,shl[6],1024);
  LD16(hs[14],rsS,g16,shl[7],0);
  LD16(hs[15],rsS,g16,shl[7],1024);
  cs4=mfma32(asb(selA),W3[25],cs4); OPQV(cs4);
  SB;
  acc[0]=mfmaL(W3[6],bf[2],acc[0]); OPQV(acc[0]);
  bf[1]=l128(b3+4608);
  SB;
  cs4=mfma32(asb(selA),W3[26],cs4); OPQV(cs4);
  SB;
  acc[0]=mfmaL(W3[7],bf[3],acc[0]); OPQV(acc[0]);
  bf[2]=l128(b3+5120);
  SB;
  cs4=mfma32(asb(selA),W3[27],cs4); OPQV(cs4);
  SB;
  acc[0]=mfmaL(W3[8],bf[0],acc[0]); OPQV(acc[0]);
  bf[3]=l128(b3+5632);
  SB;
  CHKR2(12,hs[4],hs[5],hs[6],hs[7],shl[2],shl[3]);
  *(LAS u32x4*)(hw+32768)=hs[4];
  *(LAS u32x4*)(hw+33792)=hs[5];
  acc[0]=mfmaL(W3[9],bf[1],acc[0]); OPQV(acc[0]);
  bf[0]=l128(b3+6144);
  SB;
  acc[0]=mfmaL(W3[10],bf[2],acc[0]); OPQV(acc[0]);
  bf[1]=l128(b3+6656);
  SB;
  CHK2(10,hs[6],hs[7],g16,shl[3],0,shl[3],1024);
  *(LAS u32x4*)(hw+49152)=hs[6];
  *(LAS u32x4*)(hw+50176)=hs[7];
  acc[0]=mfmaL(W3[11],bf[3],acc[0]); OPQV(acc[0]);
  bf[2]=l128(b3+7168);
  SB;
  __builtin_amdgcn_s_setprio(2);
  acc[0]=mfmaL(W3[12],bf[0],acc[0]); OPQV(acc[0]);
  bf[3]=l128(b3+7680);
  SB;
  CHK2(8,stv[0],stv[1],g16,sst,0,sst,1024);
  *(LAS f32x4*)(147456+wK+l16)=__builtin_bit_cast(f32x4,stv[0])+__builtin_bit_cast(f32x4,stv[1]);
  acc[0]=mfmaL(W3[13],bf[1],acc[0]); OPQV(acc[0]);
  bf[0]=l128(b3+8192);
  SB;
  BAR();
  W3[28]=trfrag(a3_w1_5+32768,a3_w1_5+34816);
  acc[0]=mfmaL(W3[14],bf[2],acc[0]); OPQV(acc[0]);
  bf[1]=l128(b3+8704);
  SB;
  W3[29]=trfrag(a3_w1_5+33792,a3_w1_5+35840);
  { const u32 a_=147456+g*2048+(w*16+m)*8; const f32x2 v_=lf2(a_)+lf2(a_+1024);
    const float s_=psum(v_[0]), q_=psum(v_[1]);
    const float mean=s_*(1.f/D); const float var=fmaxf(q_*(1.f/D)-mean*mean,0.f)+EPS; const float rs=__builtin_amdgcn_rsqf(var);
    *(LAS f32x4*)(155648+(w*16+m)*16)=f32x4{-2.302208198f*rs,-0.1029432397f*rs*rs*rs,var,0.f};
    const float mh_=lo2f(pk2(-mean,0.f)); const u32 pm_=pk2(mh_,-mean-mh_);
    *(LAS u32x2*)(157696+(w*16+m)*8)=u32x2{pm_,pm_}; }
  DMA(rsD,voff4,s4[0],0,wK,131072);
  acc[0]=mfmaL(W3[15],bf[3],acc[0]); OPQV(acc[0]);
  bf[2]=l128(b3+9216);
  SB;
  W3[30]=trfrag(a3_w1_5+49152,a3_w1_5+51200);
  DMA(rsD,voff4,s4[0],16384,wK,139264);
  acc[0]=mfmaL(W3[16],bf[0],acc[0]); OPQV(acc[0]);
  bf[3]=l128(b3+9728);
  SB;
  W3[31]=trfrag(a3_w1_5+50176,a3_w1_5+52224);
  acc[0]=mfmaL(W3[17],bf[1],acc[0]); OPQV(acc[0]);
  bf[0]=l128(b3+10240);
  SB;
  acc[0]=mfmaL(W3[18],bf[2],acc[0]); OPQV(acc[0]);
  bf[1]=l128(b3+10752);
  SB;
  cs4=mfma32(asb(selA),W3[28],cs4); OPQV(cs4);
  SB;
  acc[0]=mfmaL(W3[19],bf[3],acc[0]); OPQV(acc[0]);
  bf[2]=l128(b3+11264);
  SB;
  cs4=mfma32(asb(selA),W3[29],cs4); OPQV(cs4);
  SB;
  CHKR2(8,hs[8],hs[9],hs[10],hs[11],shl[4],shl[5]);
  u32 hw_w1_6=hw+65536; OPQV(hw_w1_6);
  *(LAS u32x4*)(hw_w1_6+0)=hs[8];
  *(LAS u32x4*)(hw_w1_6+1024)=hs[9];
  acc[0]=mfmaL(W3[20],bf[0],acc[0]); OPQV(acc[0]);
  bf[3]=l128(b3+11776);
  SB;
  cs4=mfma32(asb(selA),W3[30],cs4); OPQV(cs4);
  SB;
  acc[0]=mfmaL(W3[21],bf[1],acc[0]); OPQV(acc[0]);
  bf[0]=l128(b3+12288);
  SB;
  __builtin_amdgcn_s_setprio(1);
  cs4=mfma32(asb(selA),W3[31],cs4); OPQV(cs4);
  SB;
  CHK2(6,hs[10],hs[11],g16,shl[5],0,shl[5],1024);
  *(LAS u32x4*)(hw_w1_6+16384)=hs[10];
  *(LAS u32x4*)(hw_w1_6+17408)=hs[11];
  acc[0]=mfmaL(W3[22],bf[2],acc[0]); OPQV(acc[0]);
  bf[1]=l128(b3+12800);
  SB;
  acc[0]=mfmaL(W3[23],bf[3],acc[0]); OPQV(acc[0]);
  bf[2]=l128(b3+13312);
  SB;
  { u32x4 arm2={~0u,~0u,~0u,~0u}; OPQV(arm2); ST16(arm2,rsS,vhh,shh+229376); }
  BAR();
  acc[0]=mfmaL(W3[24],bf[0],acc[0]); OPQV(acc[0]);
  bf[3]=l128(b3+13824);
  SB;
  CHKR2(5,hs[12],hs[13],hs[14],hs[15],shl[6],shl[7]);
  *(LAS u32x4*)(hw_w1_6+32768)=hs[12];
  *(LAS u32x4*)(hw_w1_6+33792)=hs[13];
  acc[0]=mfmaL(W3[25],bf[1],acc[0]); OPQV(acc[0]);
  bf[0]=l128(b3+14336);
  SB;
  { u32x4 arm2={~0u,~0u,~0u,~0u}; OPQV(arm2); ST16(arm2,rsS,vhh,shh+344064); }
  acc[0]=mfmaL(W3[26],bf[2],acc[0]); OPQV(acc[0]);
  bf[1]=l128(b3+14848);
  SB;
  CHK2(4,hs[14],hs[15],g16,shl[7],0,shl[7],1024);
  *(LAS u32x4*)(hw_w1_6+49152)=hs[14];
  *(LAS u32x4*)(hw_w1_6+50176)=hs[15];
  acc[0]=mfmaL(W3[27],bf[3],acc[0]); OPQV(acc[0]);
  bf[2]=l128(b3+15360);
  SB;
  u32x4 ax, bx={0,0,0,0};
  { const float c_=hi?0.f:cs4[0]; const float ch_=lo2f(pk2(c_,0.f)), cl_=c_-ch_; ax=u32x4{pk2(ch_,ch_),pk2(cl_,cl_),0u,0u}; OPQV(ax); }
  u32 asm_=157696+(l&31)*8; OPQV(asm_);
  acc[0]=mfmaL(W3[28],bf[0],acc[0]); OPQV(acc[0]);
  bf[3]=l128(b3+15872);
  SB;
  { const u32x2 t_=*(LAS u32x2*)(asm_+0); bx[0]=t_[0]; bx[1]=t_[1]; }
  __builtin_amdgcn_s_setprio(0);
  acc[0]=mfmaL(W3[29],bf[1],acc[0]); OPQV(acc[0]);
  bf[0]=l128(b3+32768);
  SB;
  acc[0]=mfmaL(W3[30],bf[2],acc[0]); OPQV(acc[0]);
  bf[1]=l128(b3+33280);
  SB;
  u32 sf3=act3? fl+FLB+fb*64 : 0x70000000u; OPQS(sf3);
  bf16x4 W4a[28], W4b[28];
  u32 a4[4]; _Pragma("unroll") for(int nt=0;nt<4;nt++){ a4[nt]=wK+(g>>1)*8192+(4*(g&1)+q4)*128+((nt^sw1)*32)+p4*8; OPQV(a4[nt]); }
  acc[0]=mfmaL(W3[31],bf[3],acc[0]); OPQV(acc[0]);
  bf[2]=l128(b3+33792);
  SB;
  acc[0]=mfmaL(asb(ax),asb(bx),acc[0]); OPQV(acc[0]);
  SB;
  asm volatile("; MARK tile0 done");
  STAMP(12);
  acc[1]=mfmaL(W3[0],bf[0],z16); OPQV(acc[1]);
  bf[3]=l128(b3+34304);
  SB;
  BAR();
  __builtin_amdgcn_s_setprio(3);
  acc[1]=mfmaL(W3[1],bf[1],acc[1]); OPQV(acc[1]);
  bf[0]=l128(b3+34816);
  SB;
  cst=lf4(ast+0);
  DMA(rsD,voff4,s4[0],8192,wK,0);
  acc[1]=mfmaL(W3[2],bf[2],acc[1]); OPQV(acc[1]);
  bf[1]=l128(b3+35328);
  SB;
  VMUL(et[0],acc[0][0],acc[0][0]);
  VMUL(et[1],acc[0][1],acc[0][1]);
  VFMA(et[0],et[0],cst[1],cst[0]);
  DMA(rsD,voff4,s4[0],24576,wK,8192);
  acc[1]=mfmaL(W3[3],bf[3],acc[1]); OPQV(acc[1]);
  bf[2]=l128(b3+35840);
  SB;
  VFMA(et[1],et[1],cst[1],cst[0]);
  VMUL(et[0],et[0],acc[0][0]);
  VMUL(et[1],et[1],acc[0][1]);
  DMA(rsD,voff4,s4[1],0,wK,16384);
  acc[1]=mfmaL(W3[4],bf[0],acc[1]); OPQV(acc[1]);
  bf[3]=l128(b3+36352);
  SB;
  VEXP(et[0],et[0]);
  VEXP(et[1],et[1]);
  VMUL(ea[0],acc[0][0],acc[0][8]);
  DMA(rsD,voff4,s4[1],16384,wK,24576);
  acc[1]=mfmaL(W3[5],bf[1],acc[1]); OPQV(acc[1]);
  bf[0]=l128(b3+36864);
  SB;
  VMUL(ea[1],acc[0][1],acc[0][9]);
  VFMA(et[0],et[0],cst[2],cst[2]);
  VFMA(et[1],et[1],cst[2],cst[2]);
  asm volatile("s_waitcnt vmcnt(6)" :  :: "memory");
  u32 a40_w2_7=a4[0]+131072; OPQV(a40_w2_7);
  W4a[0]=tr(a40_w2_7+0);
  u32 a41_w2_8=a4[1]+131072; OPQV(a41_w2_8);
  W4a[1]=tr(a41_w2_8+0);
  u32 a42_w2_9=a4[2]+131072; OPQV(a42_w2_9);
  W4a[2]=tr(a42_w2_9+0);
  u32 a43_w2_10=a4[3]+131072; OPQV(a43_w2_10);
  W4a[3]=tr(a43_w2_10+0);
  acc[1]=mfmaL(W3[6],bf[2],acc[1]); OPQV(acc[1]);
  bf[1]=l128(b3+37376);
  SB;
  VRCP(et[0],et[0]);
  VRCP(et[1],et[1]);
  asm volatile("s_nop 0");
  acc[1]=mfmaL(W3[7],bf[3],acc[1]); OPQV(acc[1]);
  bf[2]=l128(b3+37888);
  SB;
  VMUL(ea[0],ea[0],et[0]);
  VMUL(ea[1],ea[1],et[1]);
  VCVT(hp[0],ea[0],ea[1]);
  VMUL(et[0],acc[0][2],acc[0][2]);
  acc[1]=mfmaL(W3[8],bf[0],acc[1]); OPQV(acc[1]);
  bf[3]=l128(b3+38400);
  SB;
  VMUL(et[1],acc[0][3],acc[0][3]);
  VFMA(et[0],et[0],cst[1],cst[0]);
  VFMA(et[1],et[1],cst[1],cst[0]);
  acc[1]=mfmaL(W3[9],bf[1],acc[1]); OPQV(acc[1]);
  bf[0]=l128(b3+38912);
  SB;
  VMUL(et[0],et[0],acc[0][2]);
  VMUL(et[1],et[1],acc[0][3]);
  VEXP(et[0],et[0]);
  acc[1]=mfmaL(W3[10],bf[2],acc[1]); OPQV(acc[1]);
  bf[1]=l128(b3+39424);
  SB;
  VEXP(et[1],et[1]);
  VMUL(ea[0],acc[0][2],acc[0][10]);
  VMUL(ea[1],acc[0][3],acc[0][11]);
  DMA(rsD,voff4,s4[1],8192,wK,131072);
  acc[1]=mfmaL(W3[11],bf[3],acc[1]); OPQV(acc[1]);
  bf[2]=l128(b3+39936);
  SB;
  VFMA(et[0],et[0],cst[2],cst[2]);
  VFMA(et[1],et[1],cst[2],cst[2]);
  VRCP(et[0],et[0]);
  __builtin_amdgcn_s_setprio(2);
  DMA(rsD,voff4,s4[1],24576,wK,139264);
  acc[1]=mfmaL(W3[12],bf[0],acc[1]); OPQV(acc[1]);
  bf[3]=l128(b3+40448);
  SB;
  VRCP(et[1],et[1]);
  asm volatile("s_nop 0");
  VMUL(ea[0],ea[0],et[0]);
  acc[1]=mfmaL(W3[13],bf[1],acc[1]); OPQV(acc[1]);
  bf[0]=l128(b3+40960);
  SB;
  VMUL(ea[1],ea[1],et[1]);
  VCVT(hp[1],ea[0],ea[1]);
  VMUL(et[0],acc[0][4],acc[0][4]);
  VMUL(et[1],acc[0][5],acc[0][5]);
  acc[1]=mfmaL(W3[14],bf[2],acc[1]); OPQV(acc[1]);
  bf[1]=l128(b3+41472);
  SB;
  VFMA(et[0],et[0],cst[1],cst[0]);
  VFMA(et[1],et[1],cst[1],cst[0]);
  VMUL(et[0],et[0],acc[0][4]);
  acc[1]=mfmaL(W3[15],bf[3],acc[1]); OPQV(acc[1]);
  bf[2]=l128(b3+41984);
  SB;
  VMUL(et[1],et[1],acc[0][5]);
  VEXP(et[0],et[0]);
  VEXP(et[1],et[1]);
  acc[1]=mfmaL(W3[16],bf[0],acc[1]); OPQV(acc[1]);
  bf[3]=l128(b3+42496);
  SB;
  VMUL(ea[0],acc[0][4],acc[0][12]);
  VMUL(ea[1],acc[0][5],acc[0][13]);
  VFMA(et[0],et[0],cst[2],cst[2]);
  acc[1]=mfmaL(W3[17],bf[1],acc[1]); OPQV(acc[1]);
  bf[0]=l128(b3+43008);
  SB;
  VFMA(et[1],et[1],cst[2],cst[2]);
  VRCP(et[0],et[0]);
  VRCP(et[1],et[1]);
  acc[1]=mfmaL(W3[18],bf[2],acc[1]); OPQV(acc[1]);
  bf[1]=l128(b3+43520);
  SB;
  asm volatile("s_nop 0");
  VMUL(ea[0],ea[0],et[0]);
  VMUL(ea[1],ea[1],et[1]);
  acc[1]=mfmaL(W3[19],bf[3],acc[1]); OPQV(acc[1]);
  bf[2]=l128(b3+44032);
  SB;
  VCVT(hp[2],ea[0],ea[1]);
  VMUL(et[0],acc[0][6],acc[0][6]);
  VMUL(et[1],acc[0][7],acc[0][7]);
  VFMA(et[0],et[0],cst[1],cst[0]);
  acc[1]=mfmaL(W3[20],bf[0],acc[1]); OPQV(acc[1]);
  bf[3]=l128(b3+44544);
  SB;
  VFMA(et[1],et[1],cst[1],cst[0]);
  VMUL(et[0],et[0],acc[0][6]);
  VMUL(et[1],et[1],acc[0][7]);
  acc[1]=mfmaL(W3[21],bf[1],acc[1]); OPQV(acc[1]);
  bf[0]=l128(b3+45056);
  SB;
  VEXP(et[0],et[0]);
  VEXP(et[1],et[1]);
  VMUL(ea[0],acc[0][6],acc[0][14]);
  __builtin_amdgcn_s_setprio(1);
  acc[1]=mfmaL(W3[22],bf[2],acc[1]); OPQV(acc[1]);
  bf[1]=l128(b3+45568);
  SB;
  VMUL(ea[1],acc[0][7],acc[0][15]);
  VFMA(et[0],et[0],cst[2],cst[2]);
  VFMA(et[1],et[1],cst[2],cst[2]);
  acc[1]=mfmaL(W3[23],bf[3],acc[1]); OPQV(acc[1]);
  bf[2]=l128(b3+46080);
  SB;
  VRCP(et[0],et[0]);
  VRCP(et[1],et[1]);
  asm volatile("s_nop 0");
  acc[1]=mfmaL(W3[24],bf[0],acc[1]); OPQV(acc[1]);
  bf[3]=l128(b3+46592);
  SB;
  VMUL(ea[0],ea[0],et[0]);
  VMUL(ea[1],ea[1],et[1]);
  VCVT(hp[3],ea[0],ea[1]);
  acc[1]=mfmaL(W3[25],bf[1],acc[1]); OPQV(acc[1]);
  bf[0]=l128(b3+47104);
  SB;
  { u32x4 hho={hp[0],hp[1],hp[2],hp[3]}; ST16(hho,rsS,vhh,shh+0); }
  acc[1]=mfmaL(W3[26],bf[2],acc[1]); OPQV(acc[1]);
  bf[1]=l128(b3+47616);
  SB;
  acc[1]=mfmaL(W3[27],bf[3],acc[1]); OPQV(acc[1]);
  bf[2]=l128(b3+48128);
  SB;
  acc[1]=mfmaL(W3[28],bf[0],acc[1]); OPQV(acc[1]);
  bf[3]=l128(b3+48640);
  SB;
  { const u32x2 t_=*(LAS u32x2*)(asm_+256); bx[0]=t_[0]; bx[1]=t_[1]; }
  __builtin_amdgcn_s_setprio(0);
  acc[1]=mfmaL(W3[29],bf[1],acc[1]); OPQV(acc[1]);
  u32 b3_w1_11=b3+65536; OPQV(b3_w1_11);
  bf[0]=l128(b3_w1_11+0);
  SB;
  asm volatile("s_waitcnt vmcnt(5)" :  :: "memory");
  W4b[0]=tr(a4[0]+0);
  W4b[1]=tr(a4[1]+0);
  W4b[2]=tr(a4[2]+0);
  W4b[3]=tr(a4[3]+0);
  acc[1]=mfmaL(W3[30],bf[2],acc[1]); OPQV(acc[1]);
  bf[1]=l128(b3_w1_11+512);
  SB;
  acc[1]=mfmaL(W3[31],bf[3],acc[1]); OPQV(acc[1]);
  bf[2]=l128(b3_w1_11+1024);
  SB;
  acc[1]=mfmaL(asb(ax),asb(bx),acc[1]); OPQV(acc[1]);
  SB;
  asm volatile("; MARK tile1 done");
  STAMP(13);
  acc[0]=mfmaL(W3[0],bf[0],z16); OPQV(acc[0]);
  bf[3]=l128(b3_w1_11+1536);
  SB;
  asm volatile("s_waitcnt vmcnt(0)" :  :: "memory");
  RAWBAR();
  FLAG(w,sf3+0);
  __builtin_amdgcn_s_setprio(3);
  acc[0]=mfmaL(W3[1],bf[1],acc[0]); OPQV(acc[0]);
  bf[0]=l128(b3_w1_11+2048);
  SB;
  cst=lf4(ast+512);
  W4a[4]=tr(a4[0]+16384);
  W4a[5]=tr(a4[1]+16384);
  W4a[6]=tr(a4[2]+16384);
  W4a[7]=tr(a4[3]+16384);
  DMA(rsD,voff4,s4[3],8192,wK,32768);
  acc[0]=mfmaL(W3[2],bf[2],acc[0]); OPQV(acc[0]);
  bf[1]=l128(b3_w1_11+2560);
  SB;
  VMUL(et[0],acc[1][0],acc[1][0]);
  VMUL(et[1],acc[1][1],acc[1][1]);
  VFMA(et[0],et[0],cst[1],cst[0]);
  DMA(rsD,voff4,s4[3],24576,wK,40960);
  acc[0]=mfmaL(W3[3],bf[3],acc[0]); OPQV(acc[0]);
  bf[2]=l128(b3_w1_11+3072);
  SB;
  VFMA(et[1],et[1],cst[1],cst[0]);
  VMUL(et[0],et[0],acc[1][0]);
  VMUL(et[1],et[1],acc[1][1]);
  DMA(rsD,voff4,s4[4],0,wK,49152);
  acc[0]=mfmaL(W3[4],bf[0],acc[0]); OPQV(acc[0]);
  bf[3]=l128(b3_w1_11+3584);
  SB;
  VEXP(et[0],et[0]);
  VEXP(et[1],et[1]);
  VMUL(ea[0],acc[1][0],acc[1][8]);
  DMA(rsD,voff4,s4[4],16384,wK,57344);
  acc[0]=mfmaL(W3[5],bf[1],acc[0]); OPQV(acc[0]);
  bf[0]=l128(b3_w1_11+4096);
  SB;
  VMUL(ea[1],acc[1][1],acc[1][9]);
  VFMA(et[0],et[0],cst[2],cst[2]);
  VFMA(et[1],et[1],cst[2],cst[2]);
  acc[0]=mfmaL(W3[6],bf[2],acc[0]); OPQV(acc[0]);
  bf[1]=l128(b3_w1_11+4608);
  SB;
  VRCP(et[0],et[0]);
  VRCP(et[1],et[1]);
  asm volatile("s_nop 0");
  acc[0]=mfmaL(W3[7],bf[3],acc[0]); OPQV(acc[0]);
  bf[2]=l128(b3_w1_11+5120);
  SB;
  VMUL(ea[0],ea[0],et[0]);
  VMUL(ea[1],ea[1],et[1]);
  VCVT(hp[0],ea[0],ea[1]);
  VMUL(et[0],acc[1][2],acc[1][2]);
  W4b[4]=tr(a40_w2_7+0);
  W4b[5]=tr(a41_w2_8+0);
  W4b[6]=tr(a42_w2_9+0);
  W4b[7]=tr(a43_w2_10+0);
  DMA(rsD,voff4,s4[2],0,wK,0);
  acc[0]=mfmaL(W3[8],bf[0],acc[0]); OPQV(acc[0]);
  bf[3]=l128(b3_w1_11+5632);
  SB;
  VMUL(et[1],acc[1][3],acc[1][3]);
  VFMA(et[0],et[0],cst[1],cst[0]);
  VFMA(et[1],et[1],cst[1],cst[0]);
  acc[0]=mfmaL(W3[9],bf[1],acc[0]); OPQV(acc[0]);
  bf[0]=l128(b3_w1_11+6144);
  SB;
  VMUL(et[0],et[0],acc[1][2]);
  VMUL(et[1],et[1],acc[1][3]);
  VEXP(et[0],et[0]);
  DMA(rsD,voff4,s4[2],16384,wK,8192);
  acc[0]=mfmaL(W3[10],bf[2],acc[0]); OPQV(acc[0]);
  bf[1]=l128(b3_w1_11+6656);
  SB;
  VEXP(et[1],et[1]);
  VMUL(ea[0],acc[1][2],acc[1][10]);
  VMUL(ea[1],acc[1][3],acc[1][11]);
  acc[0]=mfmaL(W3[11],bf[3],acc[0]); OPQV(acc[0]);
  bf[2]=l128(b3_w1_11+7168);
  SB;
  VFMA(et[0],et[0],cst[2],cst[2]);
  VFMA(et[1],et[1],cst[2],cst[2]);
  VRCP(et[0],et[0]);
  __builtin_amdgcn_s_setprio(2);
  DMA(rsD,voff4,s4[2],8192,wK,16384);
  acc[0]=mfmaL(W3[12],bf[0],acc[0]); OPQV(acc[0]);
  bf[3]=l128(b3_w1_11+7680);
  SB;
  VRCP(et[1],et[1]);
  asm volatile("s_nop 0");
  VMUL(ea[0],ea[0],et[0]);
  acc[0]=mfmaL(W3[13],bf[1],acc[0]); OPQV(acc[0]);
  bf[0]=l128(b3_w1_11+8192);
  SB;
  VMUL(ea[1],ea[1],et[1]);
  VCVT(hp[1],ea[0],ea[1]);
  VMUL(et[0],acc[1][4],acc[1][4]);
  VMUL(et[1],acc[1][5],acc[1][5]);
  DMA(rsD,voff4,s4[2],24576,wK,24576);
  acc[0]=mfmaL(W3[14],bf[2],acc[0]); OPQV(acc[0]);
  bf[1]=l128(b3_w1_11+8704);
  SB;
  VFMA(et[0],et[0],cst[1],cst[0]);
  VFMA(et[1],et[1],cst[1],cst[0]);
  VMUL(et[0],et[0],acc[1][4]);
  acc[0]=mfmaL(W3[15],bf[3],acc[0]); OPQV(acc[0]);
  bf[2]=l128(b3_w1_11+9216);
  SB;
  VMUL(et[1],et[1],acc[1][5]);
  VEXP(et[0],et[0]);
  VEXP(et[1],et[1]);
  DMA(rsD,voff4,s4[3],0,wK,131072);
  acc[0]=mfmaL(W3[16],bf[0],acc[0]); OPQV(acc[0]);
  bf[3]=l128(b3_w1_11+9728);
  SB;
  VMUL(ea[0],acc[1][4],acc[1][12]);
  VMUL(ea[1],acc[1][5],acc[1][13]);
  VFMA(et[0],et[0],cst[2],cst[2]);
  acc[0]=mfmaL(W3[17],bf[1],acc[0]); OPQV(acc[0]);
  bf[0]=l128(b3_w1_11+10240);
  SB;
  VFMA(et[1],et[1],cst[2],cst[2]);
  VRCP(et[0],et[0]);
  VRCP(et[1],et[1]);
  DMA(rsD,voff4,s4[3],16384,wK,139264);
  acc[0]=mfmaL(W3[18],bf[2],acc[0]); OPQV(acc[0]);
  bf[1]=l128(b3_w1_11+10752);
  SB;
  asm volatile("s_nop 0");
  VMUL(ea[0],ea[0],et[0]);
  VMUL(ea[1],ea[1],et[1]);
  acc[0]=mfmaL(W3[19],bf[3],acc[0]); OPQV(acc[0]);
  bf[2]=l128(b3_w1_11+11264);
  SB;
  VCVT(hp[2],ea[0],ea[1]);
  VMUL(et[0],acc[1][6],acc[1][6]);
  VMUL(et[1],acc[1][7],acc[1][7]);
  VFMA(et[0],et[0],cst[1],cst[0]);
  acc[0]=mfmaL(W3[20],bf[0],acc[0]); OPQV(acc[0]);
  bf[3]=l128(b3_w1_11+11776);
  SB;
  VFMA(et[1],et[1],cst[1],cst[0]);
  VMUL(et[0],et[0],acc[1][6]);
  VMUL(et[1],et[1],acc[1][7]);
  acc[0]=mfmaL(W3[21],bf[1],acc[0]); OPQV(acc[0]);
  bf[0]=l128(b3_w1_11+12288);
  SB;
  VEXP(et[0],et[0]);
  VEXP(et[1],et[1]);
  VMUL(ea[0],acc[1][6],acc[1][14]);
  __builtin_amdgcn_s_setprio(1);
  acc[0]=mfmaL(W3[22],bf[2],acc[0]); OPQV(acc[0]);
  bf[1]=l128(b3_w1_11+12800);
  SB;
  VMUL(ea[1],acc[1][7],acc[1][15]);
  VFMA(et[0],et[0],cst[2],cst[2]);
  VFMA(et[1],et[1],cst[2],cst[2]);
  acc[0]=mfmaL(W3[23],bf[3],acc[0]); OPQV(acc[0]);
  bf[2]=l128(b3_w1_11+13312);
  SB;
  VRCP(et[0],et[0]);
  VRCP(et[1],et[1]);
  asm volatile("s_nop 0");
  acc[0]=mfmaL(W3[24],bf[0],acc[0]); OPQV(acc[0]);
  bf[3]=l128(b3_w1_11+13824);
  SB;
  VMUL(ea[0],ea[0],et[0]);
  VMUL(ea[1],ea[1],et[1]);
  VCVT(hp[3],ea[0],ea[1]);
  acc[0]=mfmaL(W3[25],bf[1],acc[0]); OPQV(acc[0]);
  bf[0]=l128(b3_w1_11+14336);
  SB;
  { u32x4 hho={hp[0],hp[1],hp[2],hp[3]}; ST16(hho,rsS,vhh,shh+114688); }
  acc[0]=mfmaL(W3[26],bf[2],acc[0]); OPQV(acc[0]);
  bf[1]=l128(b3_w1_11+14848);
  SB;
  acc[0]=mfmaL(W3[27],bf[3],acc[0]); OPQV(acc[0]);
  bf[2]=l128(b3_w1_11+15360);
  SB;
  acc[0]=mfmaL(W3[28],bf[0],acc[0]); OPQV(acc[0]);
  bf[3]=l128(b3_w1_11+15872);
  SB;
  { const u32x2 t_=*(LAS u32x2*)(asm_+512); bx[0]=t_[0]; bx[1]=t_[1]; }
  __builtin_amdgcn_s_setprio(0);
  acc[0]=mfmaL(W3[29],bf[1],acc[0]); OPQV(acc[0]);
  bf[0]=l128(b3_w1_11+32768);
  SB;
  u32x4 hb[3][7];
  u32 sh4[7]; _Pragma("unroll") for(int b=0;b<7;b++){ const int k_=(b+rot7>=7)?b+rot7-7:b+rot7; sh4[b]=WS_HH+((p*8+rb4)*56+7*(w4^(4*SWHH))+k_)*1024; OPQS(sh4[b]); }
  u32 vq[4]; _Pragma("unroll") for(int q=0;q<4;q++){ vq[q]=g16+q*2*57344; OPQV(vq[q]); }
  u32 vf3=min(((7*w4)>>2)+l,(7*w4+6)>>2)*64, sq3=fl+FLB; OPQV(vf3); OPQS(sq3);
  u32 vth=(4*min(l,13)+(cb&3))*128, sth=WS_HH+((p*8+rb4)*56+7*(w4^(4*SWHH)))*1024; OPQV(vth); OPQS(sth);
  u32 vp3=FX((u32)(min(l,13)*512+496)), p2, p3; OPQV(vp3);
  u64 fq[2];
  acc[0]=mfmaL(W3[30],bf[2],acc[0]); OPQV(acc[0]);
  bf[1]=l128(b3_w1_11+33280);
  SB;
  acc[0]=mfmaL(W3[31],bf[3],acc[0]); OPQV(acc[0]);
  bf[2]=l128(b3_w1_11+33792);
  SB;
  acc[0]=mfmaL(asb(ax),asb(bx),acc[0]); OPQV(acc[0]);
  SB;
  asm volatile("; MARK tile2 done");
  STAMP(14);
  acc[1]=mfmaL(W3[0],bf[0],z16); OPQV(acc[1]);
  bf[3]=l128(b3_w1_11+34304);
  SB;
  asm volatile("s_waitcnt vmcnt(0)" :  :: "memory");
  RAWBAR();
  FLAG(w,sf3+1024);
  __builtin_amdgcn_s_setprio(3);
  acc[1]=mfmaL(W3[1],bf[1],acc[1]); OPQV(acc[1]);
  bf[0]=l128(b3_w1_11+34816);
  SB;
  cst=lf4(ast+1024);
  VMUL(et[0],acc[0][0],acc[0][0]);
  VMUL(et[1],acc[0][1],acc[0][1]);
  VFMA(et[0],et[0],cst[1],cst[0]);
  VFMA(et[1],et[1],cst[1],cst[0]);
  VMUL(et[0],et[0],acc[0][0]);
  VMUL(et[1],et[1],acc[0][1]);
  DMA(rsD,voff4,s4[4],8192,wK,65536);
  acc[1]=mfmaL(W3[2],bf[2],acc[1]); OPQV(acc[1]);
  bf[1]=l128(b3_w1_11+35328);
  SB;
  VEXP(et[0],et[0]);
  VEXP(et[1],et[1]);
  VMUL(ea[0],acc[0][0],acc[0][8]);
  VMUL(ea[1],acc[0][1],acc[0][9]);
  VFMA(et[0],et[0],cst[2],cst[2]);
  VFMA(et[1],et[1],cst[2],cst[2]);
  VRCP(et[0],et[0]);
  VRCP(et[1],et[1]);
  asm volatile("s_nop 0");
  DMA(rsD,voff4,s4[4],24576,wK,73728);
  acc[1]=mfmaL(W3[3],bf[3],acc[1]); OPQV(acc[1]);
  bf[2]=l128(b3_w1_11+35840);
  SB;
  VMUL(ea[0],ea[0],et[0]);
  VMUL(ea[1],ea[1],et[1]);
  VCVT(hp[0],ea[0],ea[1]);
  VMUL(et[0],acc[0][2],acc[0][2]);
  VMUL(et[1],acc[0][3],acc[0][3]);
  VFMA(et[0],et[0],cst[1],cst[0]);
  VFMA(et[1],et[1],cst[1],cst[0]);
  VMUL(et[0],et[0],acc[0][2]);
  VMUL(et[1],et[1],acc[0][3]);
  VEXP(et[0],et[0]);
  DMA(rsD,voff4,s4[5],0,wK,81920);
  W4a[8]=tr(a4[0]+0);
  W4a[9]=tr(a4[1]+0);
  W4a[10]=tr(a4[2]+0);
  W4a[11]=tr(a4[3]+0);
  acc[1]=mfmaL(W3[4],bf[0],acc[1]); OPQV(acc[1]);
  bf[3]=l128(b3_w1_11+36352);
  SB;
  VEXP(et[1],et[1]);
  VMUL(ea[0],acc[0][2],acc[0][10]);
  VMUL(ea[1],acc[0][3],acc[0][11]);
  VFMA(et[0],et[0],cst[2],cst[2]);
  VFMA(et[1],et[1],cst[2],cst[2]);
  VRCP(et[0],et[0]);
  VRCP(et[1],et[1]);
  asm volatile("s_nop 0");
  VMUL(ea[0],ea[0],et[0]);
  DMA(rsD,voff4,s4[5],16384,wK,90112);
  W4b[8]=tr(a4[0]+16384);
  W4b[9]=tr(a4[1]+16384);
  W4b[10]=tr(a4[2]+16384);
  W4b[11]=tr(a4[3]+16384);
  LD8(fq[0],rsS,vf3,sq3+0,0);
  acc[1]=mfmaL(W3[5],bf[1],acc[1]); OPQV(acc[1]);
  bf[0]=l128(b3_w1_11+36864);
  SB;
  VMUL(ea[1],ea[1],et[1]);
  VCVT(hp[1],ea[0],ea[1]);
  VMUL(et[0],acc[0][4],acc[0][4]);
  VMUL(et[1],acc[0][5],acc[0][5]);
  VFMA(et[0],et[0],cst[1],cst[0]);
  VFMA(et[1],et[1],cst[1],cst[0]);
  VMUL(et[0],et[0],acc[0][4]);
  VMUL(et[1],et[1],acc[0][5]);
  VEXP(et[0],et[0]);
  VEXP(et[1],et[1]);
  acc[1]=mfmaL(W3[6],bf[2],acc[1]); OPQV(acc[1]);
  bf[1]=l128(b3_w1_11+37376);
  SB;
  VMUL(ea[0],acc[0][4],acc[0][12]);
  VMUL(ea[1],acc[0][5],acc[0][13]);
  VFMA(et[0],et[0],cst[2],cst[2]);
  VFMA(et[1],et[1],cst[2],cst[2]);
  VRCP(et[0],et[0]);
  VRCP(et[1],et[1]);
  asm volatile("s_nop 0");
  VMUL(ea[0],ea[0],et[0]);
  VMUL(ea[1],ea[1],et[1]);
  acc[1]=mfmaL(W3[7],bf[3],acc[1]); OPQV(acc[1]);
  bf[2]=l128(b3_w1_11+37888);
  SB;
  VCVT(hp[2],ea[0],ea[1]);
  VMUL(et[0],acc[0][6],acc[0][6]);
  VMUL(et[1],acc[0][7],acc[0][7]);
  VFMA(et[0],et[0],cst[1],cst[0]);
  VFMA(et[1],et[1],cst[1],cst[0]);
  VMUL(et[0],et[0],acc[0][6]);
  VMUL(et[1],et[1],acc[0][7]);
  VEXP(et[0],et[0]);
  VEXP(et[1],et[1]);
  VMUL(ea[0],acc[0][6],acc[0][14]);
  W4a[12]=tr(a40_w2_7+0);
  W4a[13]=tr(a41_w2_8+0);
  W4a[14]=tr(a42_w2_9+0);
  W4a[15]=tr(a43_w2_10+0);
  acc[1]=mfmaL(W3[8],bf[0],acc[1]); OPQV(acc[1]);
  bf[3]=l128(b3_w1_11+38400);
  SB;
  VMUL(ea[1],acc[0][7],acc[0][15]);
  VFMA(et[0],et[0],cst[2],cst[2]);
  VFMA(et[1],et[1],cst[2],cst[2]);
  VRCP(et[0],et[0]);
  VRCP(et[1],et[1]);
  asm volatile("s_nop 0");
  VMUL(ea[0],ea[0],et[0]);
  VMUL(ea[1],ea[1],et[1]);
  VCVT(hp[3],ea[0],ea[1]);
  W4b[12]=tr(a4[0]+32768);
  W4b[13]=tr(a4[1]+32768);
  W4b[14]=tr(a4[2]+32768);
  W4b[15]=tr(a4[3]+32768);
  DMA(rsD,voff4,s4[5],8192,wK,0);
  acc[1]=mfmaL(W3[9],bf[1],acc[1]); OPQV(acc[1]);
  bf[0]=l128(b3_w1_11+38912);
  SB;
  { u32x4 hho={hp[0],hp[1],hp[2],hp[3]}; ST16(hho,rsS,vhh,shh+229376); }
  acc[1]=mfmaL(W3[10],bf[2],acc[1]); OPQV(acc[1]);
  bf[1]=l128(b3_w1_11+39424);
  SB;
  DMA(rsD,voff4,s4[5],24576,wK,8192);
  CHKF(3,fq[0],vf3,sq3+0);
  LD16(hb[0][0],rsS,vq[0],sh4[0],0);
  acc[1]=mfmaL(W3[11],bf[3],acc[1]); OPQV(acc[1]);
  bf[2]=l128(b3_w1_11+39936);
  SB;
  __builtin_amdgcn_s_setprio(2);
  acc[1]=mfmaL(W3[12],bf[0],acc[1]); OPQV(acc[1]);
  bf[3]=l128(b3_w1_11+40448);
  SB;
  DMA(rsD,voff4,s4[6],0,wK,16384);
  LD16(hb[0][1],rsS,vq[0],sh4[1],0);
  acc[1]=mfmaL(W3[13],bf[1],acc[1]); OPQV(acc[1]);
  bf[0]=l128(b3_w1_11+40960);
  SB;
  DMA(rsD,voff4,s4[6],16384,wK,24576);
  acc[1]=mfmaL(W3[14],bf[2],acc[1]); OPQV(acc[1]);
  bf[1]=l128(b3_w1_11+41472);
  SB;
  DMA(rsD,voff4,s4[6],8192,wK,131072);
  LD16(hb[0][2],rsS,vq[0],sh4[2],0);
  acc[1]=mfmaL(W3[15],bf[3],acc[1]); OPQV(acc[1]);
  bf[2]=l128(b3_w1_11+41984);
  SB;
  acc[1]=mfmaL(W3[16],bf[0],acc[1]); OPQV(acc[1]);
  bf[3]=l128(b3_w1_11+42496);
  SB;
  DMA(rsD,voff4,s4[6],24576,wK,139264);
  LD16(hb[0][3],rsS,vq[0],sh4[3],0);
  LD8(fq[1],rsS,vf3,sq3+1024,0);
  acc[1]=mfmaL(W3[17],bf[1],acc[1]); OPQV(acc[1]);
  bf[0]=l128(b3_w1_11+43008);
  SB;
  acc[1]=mfmaL(W3[18],bf[2],acc[1]); OPQV(acc[1]);
  bf[1]=l128(b3_w1_11+43520);
  SB;
  LD16(hb[0][4],rsS,vq[0],sh4[4],0);
  acc[1]=mfmaL(W3[19],bf[3],acc[1]); OPQV(acc[1]);
  bf[2]=l128(b3_w1_11+44032);
  SB;
  acc[1]=mfmaL(W3[20],bf[0],acc[1]); OPQV(acc[1]);
  bf[3]=l128(b3_w1_11+44544);
  SB;
  LD16(hb[0][5],rsS,vq[0],sh4[5],0);
  acc[1]=mfmaL(W3[21],bf[1],acc[1]); OPQV(acc[1]);
  bf[0]=l128(b3_w1_11+45056);
  SB;
  __builtin_amdgcn_s_setprio(1);
  acc[1]=mfmaL(W3[22],bf[2],acc[1]); OPQV(acc[1]);
  bf[1]=l128(b3_w1_11+45568);
  SB;
  LD16(hb[0][6],rsS,vq[0],sh4[6],0);
  acc[1]=mfmaL(W3[23],bf[3],acc[1]); OPQV(acc[1]);
  bf[2]=l128(b3_w1_11+46080);
  SB;
  acc[1]=mfmaL(W3[24],bf[0],acc[1]); OPQV(acc[1]);
  bf[3]=l128(b3_w1_11+46592);
  SB;
  CHKF(3,fq[1],vf3,sq3+1024);
  LD16(hb[1][0],rsS,vq[1],sh4[0],0);
  acc[1]=mfmaL(W3[25],bf[1],acc[1]); OPQV(acc[1]);
  bf[0]=l128(b3_w1_11+47104);
  SB;
  acc[1]=mfmaL(W3[26],bf[2],acc[1]); OPQV(acc[1]);
  bf[1]=l128(b3_w1_11+47616);
  SB;
  LD16(hb[1][1],rsS,vq[1],sh4[1],0);
  acc[1]=mfmaL(W3[27],bf[3],acc[1]); OPQV(acc[1]);
  bf[2]=l128(b3_w1_11+48128);
  SB;
  acc[1]=mfmaL(W3[28],bf[0],acc[1]); OPQV(acc[1]);
  bf[3]=l128(b3_w1_11+48640);
  SB;
  { const u32x2 t_=*(LAS u32x2*)(asm_+768); bx[0]=t_[0]; bx[1]=t_[1]; }
  __builtin_amdgcn_s_setprio(0);
  LD16(hb[1][2],rsS,vq[1],sh4[2],0);
  acc[1]=mfmaL(W3[29],bf[1],acc[1]); OPQV(acc[1]);
  SB;
  acc[1]=mfmaL(W3[30],bf[2],acc[1]); OPQV(acc[1]);
  SB;
  LD16(hb[1][3],rsS,vq[1],sh4[3],0);
  acc[1]=mfmaL(W3[31],bf[3],acc[1]); OPQV(acc[1]);
  SB;
  acc[1]=mfmaL(asb(ax),asb(bx),acc[1]); OPQV(acc[1]);
  SB;
  W4a[16]=tr(a4[0]+49152);
  W4a[17]=tr(a4[1]+49152);
  W4a[18]=tr(a4[2]+49152);
  W4a[19]=tr(a4[3]+49152);
  u32 a40_w1_12=a4[0]+65536; OPQV(a40_w1_12);
  W4b[16]=tr(a40_w1_12+0);
  u32 a41_w1_13=a4[1]+65536; OPQV(a41_w1_13);
  W4b[17]=tr(a41_w1_13+0);
  u32 a42_w1_14=a4[2]+65536; OPQV(a42_w1_14);
  W4b[18]=tr(a42_w1_14+0);
  u32 a43_w1_15=a4[3]+65536; OPQV(a43_w1_15);
  W4b[19]=tr(a43_w1_15+0);
  W4a[20]=tr(a40_w1_12+16384);
  W4a[21]=tr(a41_w1_13+16384);
  W4a[22]=tr(a42_w1_14+16384);
  W4a[23]=tr(a43_w1_15+16384);
  asm volatile("; MARK tile3 done");
  STAMP(15);
  f32x4 acc4[2][4]; u32x4 hr;
  u32 pw=FLO? wK+(g*16+(m^((g>>1)<<3)))*16 : wK+l16; OPQV(pw);
  u32x2 hra, hrb;
  const u32 mr=8*(w&1)+l7;
  const u32 vhr=(l3>>2)*1024+(2*(l3&1)*16+mr)*16+((l3>>1)&1)*8, hrs2=WS_H+((p*8+2*(w>>1)+rb4)*16+cb4*2)*1024;
  const u32 hrs=WS_H+((p*8+2*(w>>1)+rb4)*16+(cb4^(4*SWH))*2+(w&1))*1024;
  cst=lf4(ast+1536);
  TCHS(p2,rsS,vp3,sth+229376);
  SB;
  { float ho[8];
    _Pragma("unroll") for(int r=0;r<8;r++){ const float a_=acc[1][r], u_=acc[1][r+8];
      const float t_=__builtin_amdgcn_exp2f(fmaf(a_*a_,cst[1],cst[0])*a_);
      ho[r]=(a_*u_)*__builtin_amdgcn_rcpf(fmaf(t_,cst[2],cst[2])); }
    _Pragma("unroll") for(int i=0;i<4;i++) hp[i]=pk2(ho[2*i],ho[2*i+1]); }
  { u32x4 hho={hp[0],hp[1],hp[2],hp[3]}; ST16(hho,rsS,vhh,shh+344064); }
  SB;
  asm volatile("; MARK ep3 done");
  STAMP(16);
  asm volatile("" : "+v"(hb[0][0]) :: "memory");
  asm volatile("" : "+v"(hb[0][1]) :: "memory");
  asm volatile("" : "+v"(hb[0][2]) :: "memory");
  asm volatile("" : "+v"(hb[0][3]) :: "memory");
  if(w<4){
  asm volatile("" : "+v"(hb[0][0]) :: "memory");
  acc4[0][0]=mfma32(cat(W4a[0],W4b[0]),asb(hb[0][0]),z4);
  SB;
  acc4[0][1]=mfma32(cat(W4a[1],W4b[1]),asb(hb[0][0]),z4);
  SB;
  acc4[0][2]=mfma32(cat(W4a[2],W4b[2]),asb(hb[0][0]),z4);
  SB;
  acc4[0][3]=mfma32(cat(W4a[3],W4b[3]),asb(hb[0][0]),z4);
  SB;
  asm volatile("" : "+v"(hb[0][1]) :: "memory");
  acc4[0][0]=mfma32(cat(W4a[4],W4b[4]),asb(hb[0][1]),acc4[0][0]);
  SB;
  acc4[0][1]=mfma32(cat(W4a[5],W4b[5]),asb(hb[0][1]),acc4[0][1]);
  SB;
  acc4[0][2]=mfma32(cat(W4a[6],W4b[6]),asb(hb[0][1]),acc4[0][2]);
  SB;
  acc4[0][3]=mfma32(cat(W4a[7],W4b[7]),asb(hb[0][1]),acc4[0][3]);
  SB;
  asm volatile("" : "+v"(hb[0][2]) :: "memory");
  acc4[0][0]=mfma32(cat(W4a[8],W4b[8]),asb(hb[0][2]),acc4[0][0]);
  SB;
  acc4[0][1]=mfma32(cat(W4a[9],W4b[9]),asb(hb[0][2]),acc4[0][1]);
  SB;
  acc4[0][2]=mfma32(cat(W4a[10],W4b[10]),asb(hb[0][2]),acc4[0][2]);
  SB;
  acc4[0][3]=mfma32(cat(W4a[11],W4b[11]),asb(hb[0][2]),acc4[0][3]);
  SB;
  asm volatile("" : "+v"(hb[0][3]) :: "memory");
  acc4[0][0]=mfma32(cat(W4a[12],W4b[12]),asb(hb[0][3]),acc4[0][0]);
  SB;
  acc4[0][1]=mfma32(cat(W4a[13],W4b[13]),asb(hb[0][3]),acc4[0][1]);
  SB;
  acc4[0][2]=mfma32(cat(W4a[14],W4b[14]),asb(hb[0][3]),acc4[0][2]);
  SB;
  acc4[0][3]=mfma32(cat(W4a[15],W4b[15]),asb(hb[0][3]),acc4[0][3]);
  SB;
  RAWBAR(); } else { RAWBAR();
  asm volatile("" : "+v"(hb[0][0]) :: "memory");
  acc4[0][0]=mfma32(cat(W4a[0],W4b[0]),asb(hb[0][0]),z4);
  SB;
  acc4[0][1]=mfma32(cat(W4a[1],W4b[1]),asb(hb[0][0]),z4);
  SB;
  acc4[0][2]=mfma32(cat(W4a[2],W4b[2]),asb(hb[0][0]),z4);
  SB;
  acc4[0][3]=mfma32(cat(W4a[3],W4b[3]),asb(hb[0][0]),z4);
  SB;
  asm volatile("" : "+v"(hb[0][1]) :: "memory");
  acc4[0][0]=mfma32(cat(W4a[4],W4b[4]),asb(hb[0][1]),acc4[0][0]);
  SB;
  acc4[0][1]=mfma32(cat(W4a[5],W4b[5]),asb(hb[0][1]),acc4[0][1]);
  SB;
  acc4[0][2]=mfma32(cat(W4a[6],W4b[6]),asb(hb[0][1]),acc4[0][2]);
  SB;
  acc4[0][3]=mfma32(cat(W4a[7],W4b[7]),asb(hb[0][1]),acc4[0][3]);
  SB;
  asm volatile("" : "+v"(hb[0][2]) :: "memory");
  acc4[0][0]=mfma32(cat(W4a[8],W4b[8]),asb(hb[0][2]),acc4[0][0]);
  SB;
  acc4[0][1]=mfma32(cat(W4a[9],W4b[9]),asb(hb[0][2]),acc4[0][1]);
  SB;
  acc4[0][2]=mfma32(cat(W4a[10],W4b[10]),asb(hb[0][2]),acc4[0][2]);
  SB;
  acc4[0][3]=mfma32(cat(W4a[11],W4b[11]),asb(hb[0][2]),acc4[0][3]);
  SB;
  asm volatile("" : "+v"(hb[0][3]) :: "memory");
  acc4[0][0]=mfma32(cat(W4a[12],W4b[12]),asb(hb[0][3]),acc4[0][0]);
  SB;
  acc4[0][1]=mfma32(cat(W4a[13],W4b[13]),asb(hb[0][3]),acc4[0][1]);
  SB;
  acc4[0][2]=mfma32(cat(W4a[14],W4b[14]),asb(hb[0][3]),acc4[0][2]);
  SB;
  acc4[0][3]=mfma32(cat(W4a[15],W4b[15]),asb(hb[0][3]),acc4[0][3]);
  SB;
  }
  asm volatile("; MARK S3");
  __builtin_amdgcn_s_setprio(3);
  LD16(hb[1][4],rsS,vq[1],sh4[4],0);
  LD16(hb[1][5],rsS,vq[1],sh4[5],0);
  LD16(hb[1][6],rsS,vq[1],sh4[6],0);
  W4b[20]=tr(a4[0]+0);
  W4b[21]=tr(a4[1]+0);
  W4b[22]=tr(a4[2]+0);
  W4b[23]=tr(a4[3]+0);
  W4a[24]=tr(a4[0]+16384);
  W4a[25]=tr(a4[1]+16384);
  W4a[26]=tr(a4[2]+16384);
  W4a[27]=tr(a4[3]+16384);
  W4b[24]=tr(a40_w2_7+0);
  W4b[25]=tr(a41_w2_8+0);
  W4b[26]=tr(a42_w2_9+0);
  W4b[27]=tr(a43_w2_10+0);
  asm volatile("; MARK H13");
  SB;
  asm volatile("; MARK q1 issued");
  CHKP(4,p2,vp3,sth+229376);
  asm volatile("; MARK p2 seen");
  LD16(hb[2][0],rsS,vq[2],sh4[0],0);
  LD16(hb[2][1],rsS,vq[2],sh4[1],0);
  LD16(hb[2][2],rsS,vq[2],sh4[2],0);
  LD16(hb[2][3],rsS,vq[2],sh4[3],0);
  asm volatile("" : "+v"(hb[0][4]) :: "memory");
  acc4[0][0]=mfma32(cat(W4a[16],W4b[16]),asb(hb[0][4]),acc4[0][0]);
  SB;
  acc4[0][1]=mfma32(cat(W4a[17],W4b[17]),asb(hb[0][4]),acc4[0][1]);
  SB;
  acc4[0][2]=mfma32(cat(W4a[18],W4b[18]),asb(hb[0][4]),acc4[0][2]);
  SB;
  acc4[0][3]=mfma32(cat(W4a[19],W4b[19]),asb(hb[0][4]),acc4[0][3]);
  SB;
  LD16(hb[2][4],rsS,vq[2],sh4[4],0);
  LD16(hb[2][5],rsS,vq[2],sh4[5],0);
  LD16(hb[2][6],rsS,vq[2],sh4[6],0);
  asm volatile("" : "+v"(hb[0][5]) :: "memory");
  acc4[0][0]=mfma32(cat(W4a[20],W4b[20]),asb(hb[0][5]),acc4[0][0]);
  SB;
  acc4[0][1]=mfma32(cat(W4a[21],W4b[21]),asb(hb[0][5]),acc4[0][1]);
  SB;
  acc4[0][2]=mfma32(cat(W4a[22],W4b[22]),asb(hb[0][5]),acc4[0][2]);
  SB;
  acc4[0][3]=mfma32(cat(W4a[23],W4b[23]),asb(hb[0][5]),acc4[0][3]);
  SB;
  asm volatile("" : "+v"(hb[0][6]) :: "memory");
  acc4[0][0]=mfma32(cat(W4a[24],W4b[24]),asb(hb[0][6]),acc4[0][0]);
  SB;
  acc4[0][1]=mfma32(cat(W4a[25],W4b[25]),asb(hb[0][6]),acc4[0][1]);
  SB;
  acc4[0][2]=mfma32(cat(W4a[26],W4b[26]),asb(hb[0][6]),acc4[0][2]);
  SB;
  acc4[0][3]=mfma32(cat(W4a[27],W4b[27]),asb(hb[0][6]),acc4[0][3]);
  SB;
  asm volatile("; MARK q0 done");
  STAMP(17);
  __builtin_amdgcn_s_setprio(2);
  LD16(hr,rsS,g16,hrs,0);
  TCHS(p3,rsS,vp3,sth+344064);
  asm volatile("" : "+v"(hb[1][0]) :: "memory");
  acc4[1][0]=mfma32(cat(W4a[0],W4b[0]),asb(hb[1][0]),z4);
  SB;
  acc4[1][1]=mfma32(cat(W4a[1],W4b[1]),asb(hb[1][0]),z4);
  SB;
  acc4[1][2]=mfma32(cat(W4a[2],W4b[2]),asb(hb[1][0]),z4);
  SB;
  acc4[1][3]=mfma32(cat(W4a[3],W4b[3]),asb(hb[1][0]),z4);
  SB;
  asm volatile("" : "+v"(hb[1][1]) :: "memory");
  acc4[1][0]=mfma32(cat(W4a[4],W4b[4]),asb(hb[1][1]),acc4[1][0]);
  SB;
  acc4[1][1]=mfma32(cat(W4a[5],W4b[5]),asb(hb[1][1]),acc4[1][1]);
  SB;
  acc4[1][2]=mfma32(cat(W4a[6],W4b[6]),asb(hb[1][1]),acc4[1][2]);
  SB;
  acc4[1][3]=mfma32(cat(W4a[7],W4b[7]),asb(hb[1][1]),acc4[1][3]);
  SB;
  *(LAS f32x4*)(pw+0)=acc4[0][0];
  *(LAS f32x4*)(pw+8192)=acc4[0][1];
  *(LAS f32x4*)(pw+16384)=acc4[0][2];
  *(LAS f32x4*)(pw+24576)=acc4[0][3];
  SB;
  asm volatile("" : "+v"(hb[1][2]) :: "memory");
  acc4[1][0]=mfma32(cat(W4a[8],W4b[8]),asb(hb[1][2]),acc4[1][0]);
  SB;
  acc4[1][1]=mfma32(cat(W4a[9],W4b[9]),asb(hb[1][2]),acc4[1][1]);
  SB;
  acc4[1][2]=mfma32(cat(W4a[10],W4b[10]),asb(hb[1][2]),acc4[1][2]);
  SB;
  acc4[1][3]=mfma32(cat(W4a[11],W4b[11]),asb(hb[1][2]),acc4[1][3]);
  SB;
  asm volatile("" : "+v"(hb[1][3]) :: "memory");
  acc4[1][0]=mfma32(cat(W4a[12],W4b[12]),asb(hb[1][3]),acc4[1][0]);
  SB;
  acc4[1][1]=mfma32(cat(W4a[13],W4b[13]),asb(hb[1][3]),acc4[1][1]);
  SB;
  acc4[1][2]=mfma32(cat(W4a[14],W4b[14]),asb(hb[1][3]),acc4[1][2]);
  SB;
  acc4[1][3]=mfma32(cat(W4a[15],W4b[15]),asb(hb[1][3]),acc4[1][3]);
  SB;
  asm volatile("s_waitcnt vmcnt(11)" : "+v"(hb[1][4]) :: "memory");
  acc4[1][0]=mfma32(cat(W4a[16],W4b[16]),asb(hb[1][4]),acc4[1][0]);
  SB;
  acc4[1][1]=mfma32(cat(W4a[17],W4b[17]),asb(hb[1][4]),acc4[1][1]);
  SB;
  acc4[1][2]=mfma32(cat(W4a[18],W4b[18]),asb(hb[1][4]),acc4[1][2]);
  SB;
  acc4[1][3]=mfma32(cat(W4a[19],W4b[19]),asb(hb[1][4]),acc4[1][3]);
  SB;
  asm volatile("s_waitcnt vmcnt(10)" : "+v"(hb[1][5]) :: "memory");
  acc4[1][0]=mfma32(cat(W4a[20],W4b[20]),asb(hb[1][5]),acc4[1][0]);
  SB;
  acc4[1][1]=mfma32(cat(W4a[21],W4b[21]),asb(hb[1][5]),acc4[1][1]);
  SB;
  acc4[1][2]=mfma32(cat(W4a[22],W4b[22]),asb(hb[1][5]),acc4[1][2]);
  SB;
  acc4[1][3]=mfma32(cat(W4a[23],W4b[23]),asb(hb[1][5]),acc4[1][3]);
  SB;
  asm volatile("s_waitcnt vmcnt(9)" : "+v"(hb[1][6]) :: "memory");
  acc4[1][0]=mfma32(cat(W4a[24],W4b[24]),asb(hb[1][6]),acc4[1][0]);
  SB;
  acc4[1][1]=mfma32(cat(W4a[25],W4b[25]),asb(hb[1][6]),acc4[1][1]);
  SB;
  acc4[1][2]=mfma32(cat(W4a[26],W4b[26]),asb(hb[1][6]),acc4[1][2]);
  SB;
  acc4[1][3]=mfma32(cat(W4a[27],W4b[27]),asb(hb[1][6]),acc4[1][3]);
  SB;
  asm volatile("; MARK p3 chk");
  CHKP(0,p3,vp3,sth+344064);
  asm volatile("; MARK p3 seen");
  STAMP(18);
  LD16(hb[0][0],rsS,vq[3],sh4[0],0);
  LD16(hb[0][1],rsS,vq[3],sh4[1],0);
  LD16(hb[0][2],rsS,vq[3],sh4[2],0);
  LD16(hb[0][3],rsS,vq[3],sh4[3],0);
  LD16(hb[0][4],rsS,vq[3],sh4[4],0);
  LD16(hb[0][5],rsS,vq[3],sh4[5],0);
  LD16(hb[0][6],rsS,vq[3],sh4[6],0);
  asm volatile("; MARK q1 done");
  STAMP(19);
  __builtin_amdgcn_s_setprio(1);
  CHK1(7,hb[2][0],vq[2],sh4[0],0);
  acc4[0][0]=mfma32(cat(W4a[0],W4b[0]),asb(hb[2][0]),z4);
  SB;
  acc4[0][1]=mfma32(cat(W4a[1],W4b[1]),asb(hb[2][0]),z4);
  SB;
  acc4[0][2]=mfma32(cat(W4a[2],W4b[2]),asb(hb[2][0]),z4);
  SB;
  acc4[0][3]=mfma32(cat(W4a[3],W4b[3]),asb(hb[2][0]),z4);
  SB;
  CHK1(7,hb[2][1],vq[2],sh4[1],0);
  acc4[0][0]=mfma32(cat(W4a[4],W4b[4]),asb(hb[2][1]),acc4[0][0]);
  SB;
  acc4[0][1]=mfma32(cat(W4a[5],W4b[5]),asb(hb[2][1]),acc4[0][1]);
  SB;
  acc4[0][2]=mfma32(cat(W4a[6],W4b[6]),asb(hb[2][1]),acc4[0][2]);
  SB;
  acc4[0][3]=mfma32(cat(W4a[7],W4b[7]),asb(hb[2][1]),acc4[0][3]);
  SB;
  *(LAS f32x4*)(pw+32768)=acc4[1][0];
  *(LAS f32x4*)(pw+40960)=acc4[1][1];
  *(LAS f32x4*)(pw+49152)=acc4[1][2];
  *(LAS f32x4*)(pw+57344)=acc4[1][3];
  SB;
  CHK1(7,hb[2][2],vq[2],sh4[2],0);
  acc4[0][0]=mfma32(cat(W4a[8],W4b[8]),asb(hb[2][2]),acc4[0][0]);
  SB;
  acc4[0][1]=mfma32(cat(W4a[9],W4b[9]),asb(hb[2][2]),acc4[0][1]);
  SB;
  acc4[0][2]=mfma32(cat(W4a[10],W4b[10]),asb(hb[2][2]),acc4[0][2]);
  SB;
  acc4[0][3]=mfma32(cat(W4a[11],W4b[11]),asb(hb[2][2]),acc4[0][3]);
  SB;
  CHK1(7,hb[2][3],vq[2],sh4[3],0);
  acc4[0][0]=mfma32(cat(W4a[12],W4b[12]),asb(hb[2][3]),acc4[0][0]);
  SB;
  acc4[0][1]=mfma32(cat(W4a[13],W4b[13]),asb(hb[2][3]),acc4[0][1]);
  SB;
  acc4[0][2]=mfma32(cat(W4a[14],W4b[14]),asb(hb[2][3]),acc4[0][2]);
  SB;
  acc4[0][3]=mfma32(cat(W4a[15],W4b[15]),asb(hb[2][3]),acc4[0][3]);
  SB;
  CHK1(7,hb[2][4],vq[2],sh4[4],0);
  acc4[0][0]=mfma32(cat(W4a[16],W4b[16]),asb(hb[2][4]),acc4[0][0]);
  SB;
  acc4[0][1]=mfma32(cat(W4a[17],W4b[17]),asb(hb[2][4]),acc4[0][1]);
  SB;
  acc4[0][2]=mfma32(cat(W4a[18],W4b[18]),asb(hb[2][4]),acc4[0][2]);
  SB;
  acc4[0][3]=mfma32(cat(W4a[19],W4b[19]),asb(hb[2][4]),acc4[0][3]);
  SB;
  CHK1(7,hb[2][5],vq[2],sh4[5],0);
  acc4[0][0]=mfma32(cat(W4a[20],W4b[20]),asb(hb[2][5]),acc4[0][0]);
  SB;
  acc4[0][1]=mfma32(cat(W4a[21],W4b[21]),asb(hb[2][5]),acc4[0][1]);
  SB;
  acc4[0][2]=mfma32(cat(W4a[22],W4b[22]),asb(hb[2][5]),acc4[0][2]);
  SB;
  acc4[0][3]=mfma32(cat(W4a[23],W4b[23]),asb(hb[2][5]),acc4[0][3]);
  SB;
  CHK1(7,hb[2][6],vq[2],sh4[6],0);
  acc4[0][0]=mfma32(cat(W4a[24],W4b[24]),asb(hb[2][6]),acc4[0][0]);
  SB;
  acc4[0][1]=mfma32(cat(W4a[25],W4b[25]),asb(hb[2][6]),acc4[0][1]);
  SB;
  acc4[0][2]=mfma32(cat(W4a[26],W4b[26]),asb(hb[2][6]),acc4[0][2]);
  SB;
  acc4[0][3]=mfma32(cat(W4a[27],W4b[27]),asb(hb[2][6]),acc4[0][3]);
  SB;
  asm volatile("; MARK q2 done");
  STAMP(20);
  __builtin_amdgcn_s_setprio(0);
  CHK1(6,hb[0][0],vq[3],sh4[0],0);
  asm volatile("; MARK q3 seen");
  STAMP(21);
  acc4[1][0]=mfma32(cat(W4a[0],W4b[0]),asb(hb[0][0]),z4);
  SB;
  acc4[1][1]=mfma32(cat(W4a[1],W4b[1]),asb(hb[0][0]),z4);
  SB;
  acc4[1][2]=mfma32(cat(W4a[2],W4b[2]),asb(hb[0][0]),z4);
  SB;
  acc4[1][3]=mfma32(cat(W4a[3],W4b[3]),asb(hb[0][0]),z4);
  SB;
  CHK1(5,hb[0][1],vq[3],sh4[1],0);
  acc4[1][0]=mfma32(cat(W4a[4],W4b[4]),asb(hb[0][1]),acc4[1][0]);
  SB;
  acc4[1][1]=mfma32(cat(W4a[5],W4b[5]),asb(hb[0][1]),acc4[1][1]);
  SB;
  acc4[1][2]=mfma32(cat(W4a[6],W4b[6]),asb(hb[0][1]),acc4[1][2]);
  SB;
  acc4[1][3]=mfma32(cat(W4a[7],W4b[7]),asb(hb[0][1]),acc4[1][3]);
  SB;
  u32 pw_w1_16=pw+65536; OPQV(pw_w1_16);
  *(LAS f32x4*)(pw_w1_16+0)=acc4[0][0];
  *(LAS f32x4*)(pw_w1_16+8192)=acc4[0][1];
  *(LAS f32x4*)(pw_w1_16+16384)=acc4[0][2];
  *(LAS f32x4*)(pw_w1_16+24576)=acc4[0][3];
  SB;
  CHK1(4,hb[0][2],vq[3],sh4[2],0);
  acc4[1][0]=mfma32(cat(W4a[8],W4b[8]),asb(hb[0][2]),acc4[1][0]);
  SB;
  acc4[1][1]=mfma32(cat(W4a[9],W4b[9]),asb(hb[0][2]),acc4[1][1]);
  SB;
  acc4[1][2]=mfma32(cat(W4a[10],W4b[10]),asb(hb[0][2]),acc4[1][2]);
  SB;
  acc4[1][3]=mfma32(cat(W4a[11],W4b[11]),asb(hb[0][2]),acc4[1][3]);
  SB;
  CHK1(3,hb[0][3],vq[3],sh4[3],0);
  acc4[1][0]=mfma32(cat(W4a[12],W4b[12]),asb(hb[0][3]),acc4[1][0]);
  SB;
  acc4[1][1]=mfma32(cat(W4a[13],W4b[13]),asb(hb[0][3]),acc4[1][1]);
  SB;
  acc4[1][2]=mfma32(cat(W4a[14],W4b[14]),asb(hb[0][3]),acc4[1][2]);
  SB;
  acc4[1][3]=mfma32(cat(W4a[15],W4b[15]),asb(hb[0][3]),acc4[1][3]);
  SB;
  CHK1(2,hb[0][4],vq[3],sh4[4],0);
  acc4[1][0]=mfma32(cat(W4a[16],W4b[16]),asb(hb[0][4]),acc4[1][0]);
  SB;
  acc4[1][1]=mfma32(cat(W4a[17],W4b[17]),asb(hb[0][4]),acc4[1][1]);
  SB;
  acc4[1][2]=mfma32(cat(W4a[18],W4b[18]),asb(hb[0][4]),acc4[1][2]);
  SB;
  acc4[1][3]=mfma32(cat(W4a[19],W4b[19]),asb(hb[0][4]),acc4[1][3]);
  SB;
  CHK1(1,hb[0][5],vq[3],sh4[5],0);
  acc4[1][0]=mfma32(cat(W4a[20],W4b[20]),asb(hb[0][5]),acc4[1][0]);
  SB;
  acc4[1][1]=mfma32(cat(W4a[21],W4b[21]),asb(hb[0][5]),acc4[1][1]);
  SB;
  acc4[1][2]=mfma32(cat(W4a[22],W4b[22]),asb(hb[0][5]),acc4[1][2]);
  SB;
  acc4[1][3]=mfma32(cat(W4a[23],W4b[23]),asb(hb[0][5]),acc4[1][3]);
  SB;
  CHK1(0,hb[0][6],vq[3],sh4[6],0);
  acc4[1][0]=mfma32(cat(W4a[24],W4b[24]),asb(hb[0][6]),acc4[1][0]);
  SB;
  acc4[1][1]=mfma32(cat(W4a[25],W4b[25]),asb(hb[0][6]),acc4[1][1]);
  SB;
  acc4[1][2]=mfma32(cat(W4a[26],W4b[26]),asb(hb[0][6]),acc4[1][2]);
  SB;
  acc4[1][3]=mfma32(cat(W4a[27],W4b[27]),asb(hb[0][6]),acc4[1][3]);
  SB;
  asm volatile("; MARK q3 mfma");
  *(LAS f32x4*)(pw_w1_16+32768)=acc4[1][0];
  *(LAS f32x4*)(pw_w1_16+40960)=acc4[1][1];
  *(LAS f32x4*)(pw_w1_16+49152)=acc4[1][2];
  *(LAS f32x4*)(pw_w1_16+57344)=acc4[1][3];
  SB;
  asm volatile("" : "+v"(hr) :: "memory");
  BAR();
  asm volatile("; MARK q3 done");
  STAMP(22);
  { const u32 ra=(2*w)*8192+l16;
    f32x4 s0=f32x4{lo2f(hr[0]),hi2f(hr[0]),lo2f(hr[1]),hi2f(hr[1])}, s1=f32x4{lo2f(hr[2]),hi2f(hr[2]),lo2f(hr[3]),hi2f(hr[3])};
    if(w>=0){ _Pragma("unroll") for(int k=0;k<8;k++){ s0+=lf4(ra+k*1024); s1+=lf4(ra+8192+k*1024); } }
    const u32 a0_=pk2(s0[0],s0[1]), a1_=pk2(s0[2],s0[3]), b0_=pk2(s1[0],s1[1]), b1_=pk2(s1[2],s1[3]);
    auto e0=__builtin_amdgcn_permlane16_swap(a0_,b0_,false,false);
    auto e1=__builtin_amdgcn_permlane16_swap(a1_,b1_,false,false);
    u32x4 ov2=u32x4{(u32)e0[0],(u32)e1[0],(u32)e0[1],(u32)e1[1]};
    ST16O(ov2,rsOut,(u32)(m*1024+(g&1)*32+(g>>1)*16),(w>=0)?(u32)((p*128+32*(w>>1)+16*rb4)*1024+cb4*128+(w&1)*64):0x70000000u); }
  asm volatile("fused_hi:");
  asm volatile("; MARK end");
  STAMP(23);
}

extern "C" void kernel_launch(void* const* d_in, const int* in_sizes, int n_in, void* d_out, int out_size, void* d_ws, size_t ws_size, hipStream_t stream){
  fused<<<256,512,0,stream>>>((const u16*)d_in[0],(const u16*)d_in[1],(const u16*)d_in[2],(const u16*)d_in[4],(const u16*)d_in[5],(const u16*)d_in[6],(char*)d_ws,(u16*)d_out);
}
```
